```python
import torch

def _softmax(x, axis=-1):
    return torch.softmax(x, dim=axis)
def _gelu(x):
    return torch.nn.functional.gelu(x, approximate="tanh")
def _silu(x):
    return torch.nn.functional.silu(x)
def _sigmoid(x):
    return torch.sigmoid(x)
def _layernorm(x, axis=-1, eps=1e-5):
    m = x.mean(dim=axis, keepdim=True)
    v = x.var(dim=axis, keepdim=True, unbiased=False)
    return (x - m) / torch.sqrt(v + eps)
def _scatter_add(tgt, idx, src, axis=0):
    return tgt.clone().index_add_(axis, idx, src)
def _segment_sum(src, seg, n):
    out = torch.zeros((n,) + src.shape[1:], dtype=src.dtype)
    return out.index_add_(0, seg, src)
def _pad(x, pw):
    flat = tuple(v for pair in reversed(pw) for v in pair)
    return torch.nn.functional.pad(x, flat)

kernel_name = 'gated_mix__ssm_conv_s4__glu_gated_swiglu__prompt'

def setup_inputs():

    _w = (lambda a: (lambda t: t.to(torch.bfloat16) if t.is_floating_point() else t)(torch.as_tensor(a)))
    def _A_setup():
        B, L, D, N = 1, 2048, 512, 128
        A_re = -(lambda _x: torch.exp(torch.as_tensor(_x)))((lambda *s: torch.randn(*s, dtype=torch.bfloat16) * 3.2)(D, N))
        A_im = torch.pi * (lambda *s: torch.randn(*s, dtype=torch.bfloat16) * 0.05)(D, N)
        B_re = (lambda *s: torch.randn(*s, dtype=torch.bfloat16) * 0.1)(D, N)
        B_im = (lambda *s: torch.randn(*s, dtype=torch.bfloat16) * 0.1)(D, N)
        C_re = (lambda *s: torch.randn(*s, dtype=torch.bfloat16) * 0.1)(D, N)
        C_im = (lambda *s: torch.randn(*s, dtype=torch.bfloat16) * 0.1)(D, N)
        log_dt = (lambda *s: torch.randn(*s, dtype=torch.bfloat16) * 0.05)(D) * 0.5 - 4.0
        Dskip = (lambda *s: torch.randn(*s, dtype=torch.bfloat16) * 0.05)(D)
        return {
            "u": _w((lambda *s: torch.randn(*s, dtype=torch.bfloat16) * 0.05)(B, L, D)),
            "A_re": _w(A_re),
            "A_im": _w(A_im),
            "B_re": _w(B_re),
            "B_im": _w(B_im),
            "C_re": _w(C_re),
            "C_im": _w(C_im),
            "log_dt": _w(log_dt),
            "Dskip": _w(Dskip),
        }
    _A_in = _A_setup()

    def _B_setup():
        B, S, D, F = 1, 2048, 512, 1792
        return {
            "x": _w((lambda *s: torch.randn(*s, dtype=torch.bfloat16) * 0.05)(B, S, D)),
            "w1": _w((lambda *s: torch.randn(*s, dtype=torch.bfloat16) * 0.8)(D, F) * (2.0 / D) ** 0.5),
            "w3": _w((lambda *s: torch.randn(*s, dtype=torch.bfloat16) * 0.8)(D, F) * (2.0 / D) ** 0.5),
            "w2": _w((lambda *s: torch.randn(*s, dtype=torch.bfloat16) * 0.8)(F, D) * (2.0 / F) ** 0.5),
        }
    _B_in = _B_setup()

    return {
        "A_u": _A_in["u"],
        "A_A_re": _A_in["A_re"],
        "A_A_im": _A_in["A_im"],
        "A_B_re": _A_in["B_re"],
        "A_B_im": _A_in["B_im"],
        "A_C_re": _A_in["C_re"],
        "A_C_im": _A_in["C_im"],
        "A_log_dt": _A_in["log_dt"],
        "A_Dskip": _A_in["Dskip"],
        "B_x": _B_in["x"],
        "B_w1": _B_in["w1"],
        "B_w3": _B_in["w3"],
        "B_w2": _B_in["w2"],
        "gate_w": _w((lambda *s: torch.randn(*s, dtype=torch.bfloat16) * 0.05)(512) * 16.0),
    }

def reference(A_u, A_A_re, A_A_im, A_B_re, A_B_im, A_C_re, A_C_im, A_log_dt, A_Dskip, B_x, B_w1, B_w3, B_w2, gate_w):
    def _A_ref(u, A_re, A_im, B_re, B_im, C_re, C_im, log_dt, Dskip):
        Bsz, L, D = u.shape
        dt = torch.exp(log_dt)[:, None]
        A = A_re + 1j * A_im
        Bc = B_re + 1j * B_im
        Cc = C_re + 1j * C_im
        logdA = dt * A
        dB = dt * Bc
        l = (lambda x, dt: x.to(dt))(torch.arange(L), A_re.dtype)
        P = torch.exp(logdA[:, :, None] * l[None, None, :])
        K = torch.einsum("dn,dnl,dn->dl", Cc, P, dB).real
        L2 = 2 * L
        Kp = _pad(K, ((0, 0), (0, L)))
        up = (lambda x, *d: x.permute(*d))(u, 0, 2, 1)
        up = _pad(up, ((0, 0), (0, 0), (0, L)))
        Kf = (lambda x, axes=(-2,-1): torch.fft.rfft2(x.float(), dim=axes))(Kp[:, None, :], axes=(-2, -1))
        Uf = (lambda x, axes=(-2,-1): torch.fft.rfft2(x.float(), dim=axes))(up[:, :, None, :], axes=(-2, -1))
        Yf = Uf * Kf[None, :, :, :]
        y = (lambda x, s, axes=(-2,-1): torch.fft.irfft2(x, s=s, dim=axes).to(torch.bfloat16))(Yf, (1, L2), axes=(-2, -1))[:, :, 0, :L]
        y = (lambda x, *d: x.permute(*d))(y, 0, 2, 1) + u * Dskip[None, None, :]
        return y

    def _B_ref(x, w1, w3, w2):
        gate = torch.einsum("bsd,df->bsf", x, w1)
        up = torch.einsum("bsd,df->bsf", x, w3)
        h = _silu(gate) * up
        out = torch.einsum("bsf,fd->bsd", h, w2)
        return out

    x = A_u
    g = _sigmoid((x * gate_w).sum(-1, keepdims=True))
    a = _A_ref(x, A_A_re, A_A_im, A_B_re, A_B_im, A_C_re, A_C_im, A_log_dt, A_Dskip)
    b = _B_ref(x, B_w1, B_w3, B_w2)
    return g * a + (1 - g) * b
```

```cpp
#include <hip/hip_runtime.h>
#include <hip/hip_bf16.h>
#include <stdint.h>

typedef __bf16 bf8 __attribute__((ext_vector_type(8)));
typedef __bf16 bf2 __attribute__((ext_vector_type(2)));
typedef short s4 __attribute__((ext_vector_type(4)));
typedef short s8 __attribute__((ext_vector_type(8)));
typedef float f2 __attribute__((ext_vector_type(2)));
typedef float f4 __attribute__((ext_vector_type(4)));
typedef unsigned u2 __attribute__((ext_vector_type(2)));
typedef unsigned u4 __attribute__((ext_vector_type(4)));
typedef unsigned short us;
#define DI __device__ __forceinline__
#define LDSP(T, off) ((__attribute__((address_space(3))) T*)(uintptr_t)(off))

constexpr int L_ = 2048, D_ = 512, F_ = 1792;
constexpr size_t WS_H = 0;
constexpr size_t WS_W2T = WS_H + (size_t)F_ * L_ * 2;
constexpr size_t WS_AT = WS_W2T + (size_t)F_ * D_ * 2;
constexpr size_t WS_G = WS_AT + (size_t)D_ * L_ * 2;

DI float bf2f(unsigned v) { return __uint_as_float(v << 16); }
DI float bfhi(unsigned v) { return __uint_as_float(v & 0xffff0000u); }
DI unsigned pk2(float a, float b) { f2 v = {a, b}; return __builtin_bit_cast(unsigned, __builtin_convertvector(v, bf2)); }
DI float rbf(float a) { return bf2f(pk2(a, a) & 0xffffu); }
DI f4 mfma16(bf8 a, bf8 b, f4 c) { return __builtin_amdgcn_mfma_f32_16x16x32_bf16(a, b, c, 0, 0, 0); }
DI bf8 lds128(unsigned off) { return *LDSP(bf8, off); }
DI bf8 cat8(s4 a, s4 b) { return __builtin_bit_cast(bf8, __builtin_shufflevector(a, b, 0, 1, 2, 3, 4, 5, 6, 7)); }
DI s4 ldstr(unsigned off) { return __builtin_amdgcn_ds_read_tr16_b64_v4i16(LDSP(s4, off)); }
DI void glds16(const void* src, unsigned dst) {
  unsigned keep;
  asm volatile("s_mov_b32 %0, m0\n\ts_mov_b32 m0, %2\n\ts_nop 0\n\tglobal_load_lds_dwordx4 %1, off\n\ts_mov_b32 m0, %0"
               : "=&s"(keep) : "v"(src), "s"(dst) : "memory");
}
#define WAITVM(n) asm volatile("s_waitcnt vmcnt(" #n ")" ::: "memory")
DI void bar() { asm volatile("s_waitcnt lgkmcnt(0)\n\ts_barrier" ::: "memory"); }

struct P1 {
  const us *u, *are, *aim, *bre, *bim, *cre, *cim, *ldt, *dsk, *w1, *w3, *w2, *gw;
  char* ws;
};

constexpr int XS = 16384, WSZ = 14336;
constexpr int G1_X = 0, G1_W = 2 * XS;
constexpr int LDS1 = 79872;

DI void gemm1_role(const P1& p, int gb, unsigned sb) {
  const int tid = threadIdx.x, lane = tid & 63;
  const int wv = __builtin_amdgcn_readfirstlane(tid >> 6);
  const int xcd = gb & 7, slot = gb >> 3;
#define G1MAP 0
  const int mt = (xcd & 1) * 8 + (slot & 7), nt = (xcd >> 1) * 4 + (slot >> 3);
  const int m0 = mt * 128, n0 = nt * 112;
  const us* xsrc[4];
#pragma unroll
  for (int i = 0; i < 4; ++i) {
    int row = (wv * 4 + i) * 8 + (lane >> 3);
    xsrc[i] = p.u + (size_t)(m0 + row) * D_ + (((lane & 7) ^ (row & 7)) << 3);
  }
  const us* wsrc[4];
#pragma unroll
  for (int i = 0; i < 4; ++i) {
    int c = min((wv + 4 * i) * 64 + lane, 895);
    int k = c / 28, r = c - k * 28;
    int mat = r >= 14, jp = r - mat * 14;
    int j = jp ^ ((k >> 3) & 1);
    wsrc[i] = (mat ? p.w3 : p.w1) + (size_t)k * F_ + n0 + j * 8;
  }
  auto issueW = [&](int s) {
    unsigned dst = sb + G1_W + (s % 3) * WSZ + wv * 1024;
#pragma unroll
    for (int i = 0; i < 3; ++i) glds16(wsrc[i] + (size_t)s * 32 * F_, dst + i * 4096);
    if (wv < 2) glds16(wsrc[3] + (size_t)s * 32 * F_, dst + 3 * 4096);
  };
  auto issueX = [&](int t) {
    unsigned dst = sb + G1_X + (t & 1) * XS + wv * 4096;
#pragma unroll
    for (int i = 0; i < 4; ++i) glds16(xsrc[i] + t * 64, dst + i * 1024);
  };
  issueX(0); issueW(0); issueW(1);

  const int wm = wv & 1, wn = wv >> 1, kg = lane >> 4, r16 = lane & 15;
  unsigned xb[2];
#pragma unroll
  for (int h = 0; h < 2; ++h) xb[h] = sb + G1_X + (wm * 64 + r16) * 128 + (((h * 4 + kg) ^ (r16 & 7)) << 4);
  unsigned wbase = sb + G1_W + ((8 * kg + (r16 >> 2)) * 28 + ((lane >> 1) & 1) * 14) * 16 + (lane & 1) * 8 + wn * 112;
  const int flip = (kg & 1) * 16;
  const unsigned wb0 = wbase + (wn ? -flip : flip), wb1 = wbase + (wn ? flip : -flip);

  f4 acc[7][4];
#pragma unroll
  for (int j = 0; j < 7; ++j)
#pragma unroll
    for (int i = 0; i < 4; ++i) acc[j][i] = (f4){0.f, 0.f, 0.f, 0.f};

#pragma unroll
  for (int s = 0; s < 16; ++s) {
    if (s == 15) { WAITVM(0); }
    else if ((s & 1) && s < 14) { if (wv < 2) WAITVM(8); else WAITVM(7); }
    else { if (wv < 2) WAITVM(4); else WAITVM(3); }
    bar();
    if (s + 2 < 16) issueW(s + 2);
    if (!(s & 1) && s + 2 < 16) issueX(s / 2 + 1);
    bf8 xf[4];
#pragma unroll
    for (int i = 0; i < 4; ++i) xf[i] = lds128(xb[s & 1] + ((s >> 1) & 1) * XS + i * 2048);
#pragma unroll
    for (int j = 0; j < 7; ++j) {
      unsigned a = ((j & 1) ? wb1 : wb0) + (s % 3) * WSZ + j * 16;
      bf8 wf = cat8(ldstr(a), ldstr(a + 4 * 28 * 16));
#pragma unroll
      for (int i = 0; i < 4; ++i) acc[j][i] = mfma16(wf, xf[i], acc[j][i]);
    }
  }
  us* hws = (us*)(p.ws + WS_H);
#pragma unroll
  for (int j = 0; j < 7; ++j)
#pragma unroll
    for (int ip = 0; ip < 2; ++ip) {
      float hv[4];
#pragma unroll
      for (int r = 0; r < 4; ++r) {
        auto sw = __builtin_amdgcn_permlane32_swap(__float_as_uint(acc[j][2 * ip][r]), __float_as_uint(acc[j][2 * ip + 1][r]), false, false);
        unsigned gu = pk2(__uint_as_float(sw[0]), __uint_as_float(sw[1]));
        float g = bf2f(gu & 0xffffu), up = bfhi(gu);
        float sl = rbf(g * __builtin_amdgcn_rcpf(1.f + __expf(-g)));
        hv[r] = sl * up;
      }
      int tok = m0 + wm * 64 + (2 * ip + (lane >> 5)) * 16 + r16;
      int chunk = nt * 14 + wn * 7 + j;
      u2 o = {pk2(hv[0], hv[1]), pk2(hv[2], hv[3])};
      *(u2*)(hws + ((size_t)chunk * L_ + tok) * 8 + (kg & 1) * 4) = o;
    }
}

constexpr int TROW = 544, TCH = 53 * TROW;
constexpr int S_U = 0, UPH = 1184, UCH = 4 * UPH;
constexpr int S_KV = 2 * UCH, KVCH = 1312;
constexpr int S_T = S_KV + 2 * KVCH;
constexpr int KCP = 4128, KCH = 4 * KCP;
static_assert(S_T % 16 == 0 && S_T + TCH + 64 * TROW <= LDS1 && S_T + 2 * KCH <= LDS1, "lds");
static_assert(G1_W + 3 * WSZ <= LDS1, "lds");

DI f2 cmul(f2 a, f2 b) { return (f2){fmaf(a.x, b.x, -a.y * b.y), fmaf(a.x, b.y, a.y * b.x)}; }
DI f2 cexpl(float ar, float ai, float l) {
  float e = __expf(ar * l);
  float x = ai * l;
  float r = x * 0.15915494309189535f;
  float err = fmaf(x, 0.15915494309189535f, -r) + x * 6.4206383e-9f;
  float f = __builtin_amdgcn_fractf(r) + err;
  return (f2){e * __builtin_amdgcn_cosf(f), e * __builtin_amdgcn_sinf(f)};
}

template <int Q0, int Q1, int Q2, int Q3>
DI void conv_wave(unsigned kb, unsigned ub, f4 (&acc)[4]) {
  constexpr int QS[4] = {Q0, Q1, Q2, Q3};
#pragma unroll 1
  for (int rho = 0; rho < 4; ++rho) {
    bf8 uf[2 * Q0 + 2];
#pragma unroll
    for (int s = 0; s < 2 * Q0 + 2; ++s) {
      unsigned a = ub + rho * UPH + s * 64;
      uf[s] = cat8(*LDSP(s4, a), *LDSP(s4, a + 8));
    }
#pragma unroll
    for (int t = 0; t < 2 * Q0 + 2; ++t) {
      unsigned a = kb + rho * KCP + t * 256;
      bf8 kf = cat8(ldstr(a + 32), ldstr(a));
#pragma unroll
      for (int i = 0; i < 4; ++i)
        if (t <= 2 * QS[i] + 1) acc[i] = mfma16(kf, uf[2 * QS[i] - t + 1], acc[i]);
    }
  }
}

DI void ssm_role(const P1& p, int b, unsigned sb) {
  const int tid = threadIdx.x, lane = tid & 63;
  const int wv = __builtin_amdgcn_readfirstlane(tid >> 6);
  const int d0 = 64 * (b & 7) + 2 * (b >> 3);
  const int ch = wv >> 1, n = tid & 127, kg = lane >> 4, r16 = lane & 15;
  const int pi = (d0 + ch) * 128 + n;
  const float Are = bf2f(p.are[pi]), Aim = bf2f(p.aim[pi]), Bre = bf2f(p.bre[pi]), Bim = bf2f(p.bim[pi]);
  const float Cre = bf2f(p.cre[pi]), Cim = bf2f(p.cim[pi]);
  const float ldt = bf2f(p.ldt[d0 + ch]), dsk = bf2f(p.dsk[d0 + ch]);
  unsigned uv[8];
#pragma unroll
  for (int i = 0; i < 4; ++i) {
    int l = 8 * ((tid >> 2) + 64 * i) + (tid & 3);
    uv[2 * i] = *(const unsigned*)(p.u + (size_t)l * D_ + d0);
    uv[2 * i + 1] = *(const unsigned*)(p.u + (size_t)(l + 4) * D_ + d0);
  }
  u4 gx[2], gwv;
  gwv = *(const u4*)(p.gw + lane * 8);
#pragma unroll
  for (int i = 0; i < 2; ++i) gx[i] = *(const u4*)(p.u + (size_t)(b * 8 + wv * 2 + i) * D_ + lane * 8);
  unsigned w2v[8];
  if (b < 224) {
#pragma unroll
    for (int i = 0; i < 8; ++i) w2v[i] = *(const unsigned*)(p.w2 + (size_t)(b * 8 + i) * D_ + tid * 2);
  }
  {
    const float dt = rbf(__expf(ldt));
    const float ar = dt * Are, ai = dt * Aim;
    const f2 W = cmul((f2){Cre, Cim}, (f2){dt * Bre, dt * Bim});
    const f2 e1 = cexpl(ar, ai, 1.f), e8 = cexpl(ar, ai, 8.f), e128 = cexpl(ar, ai, 128.f), e256 = cexpl(ar, ai, 256.f);
    const unsigned tb = sb + S_T + ch * TCH + n * 4;
    f2 lo = {1.f, 0.f};
#pragma unroll
    for (int i = 0; i < 16; ++i) {
      f2 hi = cmul(lo, e128);
      *LDSP(unsigned, tb + i * TROW) = pk2(lo.x, lo.y);
      *LDSP(unsigned, tb + (16 + i) * TROW) = pk2(hi.x, hi.y);
      lo = cmul(lo, e8);
    }
    f2 a[8];
    a[0] = W;
#pragma unroll
    for (int i = 1; i < 8; ++i) a[i] = cmul(a[i - 1], e1);
#define PUTA(row, v) *LDSP(unsigned, tb + (32 + (row)) * TROW) = pk2((v).x, -(v).y)
#pragma unroll
    for (int i = 0; i < 8; ++i) PUTA(i, a[i]);
#pragma unroll
    for (int i = 0; i < 4; ++i) { a[2 * i] = cmul(a[2 * i], e256); PUTA(8 + i, a[2 * i]); }
    a[0] = cmul(a[0], e256); a[4] = cmul(a[4], e256); PUTA(12, a[0]); PUTA(13, a[4]);
    a[0] = cmul(a[0], e256); a[4] = cmul(a[4], e256); PUTA(14, a[0]); PUTA(15, a[4]);
#pragma unroll
    for (int i = 0; i < 5; ++i) { a[0] = cmul(a[0], e256); PUTA(16 + i, a[0]); }
  }
#pragma unroll
  for (int i = 0; i < 4; ++i) {
    unsigned a = sb + S_U + (tid & 3) * UPH + 128 + ((tid >> 2) + 64 * i) * 4;
    *LDSP(unsigned, a) = __builtin_amdgcn_perm(uv[2 * i + 1], uv[2 * i], 0x05040100);
    *LDSP(unsigned, a + UCH) = __builtin_amdgcn_perm(uv[2 * i + 1], uv[2 * i], 0x07060302);
  }
  *LDSP(unsigned, sb + S_U + (tid >> 5) * UPH + (tid & 31) * 4) = 0u;
  __syncthreads();
  {
    f4 k0 = {0.f, 0.f, 0.f, 0.f}, k1 = k0;
    const unsigned tb = sb + S_T + ch * TCH + r16 * TROW + kg * 16;
    const unsigned lb = tb + (wv & 1) * 16 * TROW;
#pragma unroll
    for (int ks = 0; ks < 8; ++ks) {
      bf8 bl = lds128(lb + ks * 64);
      k0 = mfma16(lds128(tb + 32 * TROW + ks * 64), bl, k0);
      k1 = mfma16(lds128(tb + 48 * TROW + ks * 64), bl, k1);
    }
    const int c = (wv & 1) * 16 + r16;
    const unsigned kv = sb + S_KV + ch * KVCH;
    int i0 = kg < 2 ? 8 * c + 4 * kg : (kg == 2 ? 256 + 4 * c : 384 + 2 * c);
    int i1 = kg < 3 ? i0 + 2 : i0 + 64;
    *LDSP(unsigned, kv + i0 * 2) = pk2(k0[0], k0[1]);
    *LDSP(unsigned, kv + i1 * 2) = pk2(k0[2], k0[3]);
    if (kg == 0) {
#pragma unroll
      for (int r = 0; r < 4; ++r) *LDSP(us, kv + (512 + 32 * r + c) * 2) = (us)pk2(k1[r], 0.f);
    } else if (kg == 1 && c == 0) {
      *LDSP(unsigned, kv + 640 * 2) = pk2(k1[0], 0.f);
    }
  }
  {
#pragma unroll
    for (int i = 0; i < 2; ++i) {
      float s = 0.f;
#pragma unroll
      for (int e = 0; e < 4; ++e) {
        unsigned pr = pk2(bf2f(gx[i][e] & 0xffffu) * bf2f(gwv[e] & 0xffffu), bfhi(gx[i][e]) * bfhi(gwv[e]));
        s += bf2f(pr & 0xffffu) + bfhi(pr);
      }
#pragma unroll
      for (int o = 32; o; o >>= 1) s += __shfl_xor(s, o);
      s = rbf(s);
      if (lane == 0) ((float*)(p.ws + WS_G))[b * 8 + wv * 2 + i] = rbf(__builtin_amdgcn_rcpf(1.f + __expf(-s)));
    }
    if (b < 224) {
      u4 lo, hi;
#pragma unroll
      for (int e = 0; e < 4; ++e) {
        lo[e] = __builtin_amdgcn_perm(w2v[2 * e + 1], w2v[2 * e], 0x05040100);
        hi[e] = __builtin_amdgcn_perm(w2v[2 * e + 1], w2v[2 * e], 0x07060302);
      }
      u4* dst = (u4*)(p.ws + WS_W2T) + (size_t)b * D_ + tid * 2;
      dst[0] = lo; dst[1] = hi;
    }
  }
  __syncthreads();
  {
    int idx[11];
#pragma unroll
    for (int e = 0; e < 11; ++e) {
      int j = 8 * tid - 3 + e;
      int v = (int)rbf((float)j);
      int ix = v < 256 ? v : (v < 512 ? 128 + (v >> 1) : (v < 1024 ? 256 + (v >> 2) : 384 + (v >> 3)));
      idx[e] = j < 0 ? 641 : ix;
    }
#pragma unroll
    for (int c = 0; c < 2; ++c) {
      unsigned v[11];
#pragma unroll
      for (int e = 0; e < 11; ++e) v[e] = *LDSP(us, sb + S_KV + c * KVCH + idx[e] * 2);
      unsigned ev[5], od[5];
#pragma unroll
      for (int i = 0; i < 5; ++i) { ev[i] = v[2 * i + 1] | (v[2 * i + 2] << 16); od[i] = v[2 * i] | (v[2 * i + 1] << 16); }
      unsigned a = sb + S_T + c * KCH + 32 + tid * 16;
      *LDSP(u4, a) = (u4){ev[1], ev[2], ev[3], ev[4]};
      *LDSP(u4, a + KCP) = (u4){od[1], od[2], od[3], od[4]};
      *LDSP(u4, a + 2 * KCP) = (u4){ev[0], ev[1], ev[2], ev[3]};
      *LDSP(u4, a + 3 * KCP) = (u4){od[0], od[1], od[2], od[3]};
    }
    if (tid < 64) *LDSP(unsigned, sb + S_T + (tid >> 3) * KCP + (tid & 7) * 4) = 0u;
  }
  __syncthreads();
  f4 acc[4];
#pragma unroll
  for (int i = 0; i < 4; ++i) acc[i] = (f4){0.f, 0.f, 0.f, 0.f};
  const unsigned kb = sb + S_T + ch * KCH + (16 + 112 - 4 * (8 * kg + 4 + (r16 >> 2)) + 4 * (lane & 3)) * 2;
  const unsigned ub = sb + S_U + ch * UCH + (64 - 32 - 28 + 4 * r16 + 8 * kg) * 2;
  int QS[4];
  if (wv & 1) { conv_wave<6, 5, 3, 0>(kb, ub, acc); QS[0] = 6; QS[1] = 5; QS[2] = 3; QS[3] = 0; }
  else { conv_wave<7, 4, 2, 1>(kb, ub, acc); QS[0] = 7; QS[1] = 4; QS[2] = 2; QS[3] = 1; }
  us* aT = (us*)(p.ws + WS_AT) + (size_t)(d0 + ch) * L_;
#pragma unroll
  for (int i = 0; i < 4; ++i) {
    float o[4];
#pragma unroll
    for (int r = 0; r < 4; ++r) {
      float uu = bf2f(*LDSP(us, sb + S_U + ch * UCH + r * UPH + (64 + 64 * QS[i] + 4 * r16 + kg) * 2));
      unsigned pr = pk2(acc[i][r], uu * dsk);
      o[r] = bf2f(pr & 0xffffu) + bfhi(pr);
    }
    *(u2*)(aT + 256 * QS[i] + 16 * r16 + 4 * kg) = (u2){pk2(o[0], o[1]), pk2(o[2], o[3])};
  }
}

__global__ void __launch_bounds__(256, 2) k_stage1(P1 p) {
  __shared__ __attribute__((aligned(16))) char smem[LDS1];
  const unsigned sb = (unsigned)(uintptr_t)LDSP(char, 0) + (unsigned)(uintptr_t)(__attribute__((address_space(3))) char*)smem;
  if (blockIdx.x < 256) gemm1_role(p, blockIdx.x, sb);
  else ssm_role(p, blockIdx.x - 256, sb);
}

constexpr int R_ROW = 272, R_TILE = 16 * R_ROW;
constexpr int G2_A = 0, G2_R = 8192, LDS2 = G2_R + 16 * R_TILE;
#define G2D 10

__global__ void __launch_bounds__(256, 1) k_stage2(const char* __restrict__ ws, us* __restrict__ out) {
  __shared__ __attribute__((aligned(16))) char smem[LDS2];
  const unsigned sb = (unsigned)(uintptr_t)(__attribute__((address_space(3))) char*)smem;
  const int tid = threadIdx.x, lane = tid & 63, kg = lane >> 4, r16 = lane & 15;
  const int wv = __builtin_amdgcn_readfirstlane(tid >> 6);
  const int xcd = blockIdx.x & 7, slot = blockIdx.x >> 3;
  const int m0 = (4 * xcd + (slot & 3)) * 64, n0 = (slot >> 2) * 64;
  const bf8* hp = (const bf8*)(ws + WS_H) + (size_t)(wv * 56 + kg) * L_ + m0 + r16;
  const bf8* wp = (const bf8*)(ws + WS_W2T) + (size_t)(wv * 56 + kg) * D_ + n0 + r16;
  bf8 hf[14][4], wf[14][4];
  auto ld = [&](int s) {
#pragma unroll
    for (int i = 0; i < 4; ++i) { wf[s][i] = wp[(size_t)s * 4 * D_ + i * 16]; hf[s][i] = hp[(size_t)s * 4 * L_ + i * 16]; }
  };
#pragma unroll
  for (int s = 0; s < G2D; ++s) ld(s);
  u4 av[2];
#pragma unroll
  for (int i = 0; i < 2; ++i)
    av[i] = *(const u4*)((const us*)(ws + WS_AT) + (size_t)(n0 + i * 32 + (tid >> 3)) * L_ + m0 + (tid & 7) * 8);
  const float g = ((const float*)(ws + WS_G))[m0 + wv * 16 + r16];
  f4 acc[4][4];
#pragma unroll
  for (int i = 0; i < 4; ++i)
#pragma unroll
    for (int j = 0; j < 4; ++j) acc[i][j] = (f4){0.f, 0.f, 0.f, 0.f};
#pragma unroll
  for (int s = 0; s < 14; ++s) {
    __builtin_amdgcn_sched_barrier(0);
#pragma unroll
    for (int i = 0; i < 4; ++i)
#pragma unroll
      for (int j = 0; j < 4; ++j) acc[i][j] = mfma16(wf[s][i], hf[s][j], acc[i][j]);
    __builtin_amdgcn_sched_barrier(0);
    if (s + G2D < 14) ld(s + G2D);
  }
#pragma unroll
  for (int i = 0; i < 2; ++i) *LDSP(u4, sb + G2_A + (i * 32 + (tid >> 3)) * 128 + (tid & 7) * 16) = av[i];
#pragma unroll
  for (int j = 0; j < 4; ++j)
#pragma unroll
    for (int i = 0; i < 4; ++i)
      *LDSP(f4, sb + G2_R + (j * 4 + wv) * R_TILE + r16 * R_ROW + (16 * i + 4 * kg) * 4) = acc[i][j];
  __syncthreads();
  const float omg = rbf(1.f - g);
#pragma unroll
  for (int i = 0; i < 4; ++i) {
    f4 s = {0.f, 0.f, 0.f, 0.f};
#pragma unroll
    for (int w = 0; w < 4; ++w) s += *LDSP(f4, sb + G2_R + (wv * 4 + w) * R_TILE + r16 * R_ROW + (16 * i + 4 * kg) * 4);
    s4 a = ldstr(sb + G2_A + (16 * i + 4 * kg + (r16 >> 2)) * 128 + (wv * 16 + 4 * (lane & 3)) * 2);
    float o[4];
#pragma unroll
    for (int r = 0; r < 4; ++r) {
      unsigned pr = pk2(g * bf2f((us)a[r]), omg * rbf(s[r]));
      o[r] = bf2f(pr & 0xffffu) + bfhi(pr);
    }
    *(u2*)(out + (size_t)(m0 + wv * 16 + r16) * D_ + n0 + 16 * i + 4 * kg) = (u2){pk2(o[0], o[1]), pk2(o[2], o[3])};
  }
}

extern "C" void kernel_launch(void* const* d_in, const int* in_sizes, int n_in, void* d_out, int out_size,
                              void* d_ws, size_t ws_size, hipStream_t stream) {
  P1 p;
  p.u = (const us*)d_in[0]; p.are = (const us*)d_in[1]; p.aim = (const us*)d_in[2];
  p.bre = (const us*)d_in[3]; p.bim = (const us*)d_in[4]; p.cre = (const us*)d_in[5]; p.cim = (const us*)d_in[6];
  p.ldt = (const us*)d_in[7]; p.dsk = (const us*)d_in[8];
  p.w1 = (const us*)d_in[10]; p.w3 = (const us*)d_in[11]; p.w2 = (const us*)d_in[12]; p.gw = (const us*)d_in[13];
  p.ws = (char*)d_ws;
  k_stage1<<<512, 256, 0, stream>>>(p);
  k_stage2<<<256, 256, 0, stream>>>((const char*)d_ws, (us*)d_out);
}
```

```cpp
#include <hip/hip_runtime.h>
#include <hip/hip_bf16.h>
#include <stdint.h>

typedef __bf16 bf8 __attribute__((ext_vector_type(8)));
typedef __bf16 bf2 __attribute__((ext_vector_type(2)));
typedef short s4 __attribute__((ext_vector_type(4)));
typedef short s8 __attribute__((ext_vector_type(8)));
typedef float f2 __attribute__((ext_vector_type(2)));
typedef float f4 __attribute__((ext_vector_type(4)));
typedef unsigned u2 __attribute__((ext_vector_type(2)));
typedef unsigned u4 __attribute__((ext_vector_type(4)));
typedef unsigned short us;
#define DI __device__ __forceinline__
#define LDSP(T, off) ((__attribute__((address_space(3))) T*)(uintptr_t)(off))

constexpr int L_ = 2048, D_ = 512, F_ = 1792;
constexpr int KVB = 2624;
constexpr size_t WS_CNT = 0;
constexpr size_t WS_H = 4096;
constexpr size_t WS_W2T = WS_H + (size_t)F_ * L_ * 2;
constexpr size_t WS_AT = WS_W2T + (size_t)F_ * D_ * 2;
constexpr size_t WS_UT = WS_AT + (size_t)D_ * L_ * 2;
constexpr size_t WS_G = WS_UT + (size_t)D_ * L_ * 2;
constexpr size_t WS_KV = WS_G + (size_t)L_ * 4;

DI float bf2f(unsigned v) { return __uint_as_float(v << 16); }
DI float bfhi(unsigned v) { return __uint_as_float(v & 0xffff0000u); }
DI unsigned pk2(float a, float b) { f2 v = {a, b}; return __builtin_bit_cast(unsigned, __builtin_convertvector(v, bf2)); }
DI float rbf(float a) { return bf2f(pk2(a, a) & 0xffffu); }
DI f4 mfma16(bf8 a, bf8 b, f4 c) { return __builtin_amdgcn_mfma_f32_16x16x32_bf16(a, b, c, 0, 0, 0); }
DI bf8 lds128(unsigned off) { return *LDSP(bf8, off); }
DI bf8 cat8(s4 a, s4 b) { return __builtin_bit_cast(bf8, __builtin_shufflevector(a, b, 0, 1, 2, 3, 4, 5, 6, 7)); }
DI s4 ldstr(unsigned off) { return __builtin_amdgcn_ds_read_tr16_b64_v4i16(LDSP(s4, off)); }
DI void glds16(const void* src, unsigned dst) {
  unsigned keep;
  asm volatile("s_mov_b32 %0, m0\n\ts_mov_b32 m0, %2\n\ts_nop 0\n\tglobal_load_lds_dwordx4 %1, off\n\ts_mov_b32 m0, %0"
               : "=&s"(keep) : "v"(src), "s"(dst) : "memory");
}
#define STH(ptr, v) (*(ptr) = (v))
#define WAITVM(n) asm volatile("s_waitcnt vmcnt(" #n ")" ::: "memory")
DI void bar() { asm volatile("s_waitcnt lgkmcnt(0)\n\ts_barrier" ::: "memory"); }

#define STAMP(ws, i) do {} while (0)
#define KID 1
struct P1 {
  const us *u, *are, *aim, *bre, *bim, *cre, *cim, *ldt, *w1, *w3, *w2, *gw;
  char* ws;
};
DI u4 swap16(u2 a, u2 b) {
  auto x = __builtin_amdgcn_permlane16_swap(a[0], b[0], false, false);
  auto y = __builtin_amdgcn_permlane16_swap(a[1], b[1], false, false);
  return (u4){x[0], y[0], x[1], y[1]};
}

constexpr int XS = 16384, WSZ = 16384;
constexpr int G1_X = 0, G1_W = 2 * XS;
constexpr int LDS1 = 81920;
#define G1MAP 1
#define ROT 1

DI void gemm1_role(const P1& p, int gb, unsigned sb) {
  const int tid = threadIdx.x, lane = tid & 63;
  const int wv = __builtin_amdgcn_readfirstlane(tid >> 6);
  const int xcd = gb & 7, slot = gb >> 3;
  const int mt = 2 * xcd + (slot & 1), nt = slot >> 1;
  const int m0 = mt * 128, n0 = nt * 128;
  const int rot = ROT == 1 ? (slot & 7) : (ROT == 2 ? ((slot >> 1) & 7) : (ROT == 3 ? xcd : 0));
  STAMP(p.ws, 0);
  const us* xsrc = p.u + (size_t)(m0 + wv * 32 + (lane >> 3)) * D_ + (((lane & 7) ^ (lane >> 3)) << 3);
  auto issueX1 = [&](int t, int i) { glds16(xsrc + ((t + rot) & 7) * 64 + i * 8 * D_, sb + G1_X + (t & 1) * XS + wv * 4096 + i * 1024); };
#pragma unroll
  for (int i = 0; i < 4; ++i) issueX1(0, i);
  const us* wsrc[2];
#pragma unroll
  for (int i = 0; i < 2; ++i) {
    int k = 8 * wv + 2 * i + (lane >> 5), f = (k & 3) | ((wv & 1) << 2);
    wsrc[i] = ((lane & 16) ? p.w3 : p.w1) + k * F_ + n0 + (((lane & 15) ^ (f << 1)) << 3);
  }
  auto issueW1 = [&](int s, int i) {
    const int ko = (((s + 2 * rot) & 15) * 32 + (i >> 1) * 4) * F_;
    glds16(wsrc[i & 1] + ko, sb + G1_W + (s % 3) * WSZ + wv * 4096 + i * 1024);
  };
#pragma unroll
  for (int i = 0; i < 4; ++i) issueW1(0, i);
#pragma unroll
  for (int i = 0; i < 4; ++i) issueW1(1, i);
#pragma unroll
  for (int i = 0; i < 4; ++i) issueX1(1, i);
#pragma unroll
  for (int i = 0; i < 4; ++i) issueW1(2, i);

  const int wm = wv & 1, wn = wv >> 1, kg = lane >> 4, r16 = lane & 15;
  unsigned xb[2], wb[4];
#pragma unroll
  for (int h = 0; h < 2; ++h) xb[h] = sb + G1_X + (wm * 64 + r16) * 128 + (((h * 4 + kg) ^ (r16 & 7)) << 4);
#pragma unroll
  for (int j = 0; j < 4; ++j)
    wb[j] = sb + G1_W + (8 * kg + (r16 >> 2)) * 512 + (((wn * 4 + j) ^ ((r16 >> 2) | ((kg & 1) << 2))) << 5) + (lane & 3) * 8;
  auto rdX = [&](int s, int i) { return lds128(xb[s & 1] + ((s >> 1) & 1) * XS + i * 2048); };
  auto rdW = [&](int s, int j) {
    unsigned a = wb[j >> 1] + (s % 3) * WSZ + (j & 1) * 256;
    return cat8(ldstr(a), ldstr(a + 2048));
  };
  f4 acc[8][4];
#pragma unroll
  for (int j = 0; j < 8; ++j)
#pragma unroll
    for (int i = 0; i < 4; ++i) acc[j][i] = (f4){0.f, 0.f, 0.f, 0.f};
  bf8 xf[2][4], wf[2][8];
  WAITVM(12);
  bar();
  STAMP(p.ws, 1);
#pragma unroll
  for (int i = 0; i < 4; ++i) xf[0][i] = rdX(0, i);
#pragma unroll
  for (int j = 0; j < 8; ++j) wf[0][j] = rdW(0, j);
#pragma unroll
  for (int s = 0; s < 16; ++s) {
    if (s < 15) {
      if (s == 14) WAITVM(0);
      else if (!(s & 1)) WAITVM(8);
      else WAITVM(4);
      bar();
    }
    __builtin_amdgcn_sched_barrier(0);
#pragma unroll
    for (int j = 0; j < 8; ++j) {
      if (s < 15) {
        if (j < 4) xf[(s + 1) & 1][j] = rdX(s + 1, j);
        wf[(s + 1) & 1][j] = rdW(s + 1, j);
      }
#pragma unroll
      for (int i = 0; i < 2; ++i) acc[j][i] = mfma16(wf[s & 1][j], xf[s & 1][i], acc[j][i]);
      __builtin_amdgcn_sched_barrier(0);
      if (j < 4) { if (s + 3 < 16) issueW1(s + 3, j); }
      else if ((s & 1) && s <= 11) issueX1((s + 3) / 2, j - 4);
#pragma unroll
      for (int i = 2; i < 4; ++i) acc[j][i] = mfma16(wf[s & 1][j], xf[s & 1][i], acc[j][i]);
      __builtin_amdgcn_sched_barrier(0);
    }
  }
  STAMP(p.ws, 7);
  u4* hws = (u4*)(p.ws + WS_H) + ((size_t)(nt * 4 + wn * 2) * 128 + mt * 8 + wm * 4 + (kg & 1)) * 64 + (kg >> 1) * 16 + r16;
#pragma unroll
  for (int j = 0; j < 4; ++j)
#pragma unroll
    for (int ip = 0; ip < 2; ++ip) {
      u2 o[2];
#pragma unroll
      for (int t = 0; t < 2; ++t)
#pragma unroll
        for (int r = 0; r < 2; ++r) {
          f2 g = {acc[2 * j][2 * ip + t][2 * r], acc[2 * j][2 * ip + t][2 * r + 1]};
          f2 up = {acc[2 * j + 1][2 * ip + t][2 * r], acc[2 * j + 1][2 * ip + t][2 * r + 1]};
          f2 e = g * -1.4426950408889634f;
          e = (f2){__builtin_amdgcn_exp2f(e.x), __builtin_amdgcn_exp2f(e.y)} + 1.f;
          e = (g * (f2){__builtin_amdgcn_rcpf(e.x), __builtin_amdgcn_rcpf(e.y)}) * up;
          o[t][r] = pk2(e.x, e.y);
        }
      STH(hws + ((size_t)(j >> 1) * 128 + ip * 2) * 64 + (j & 1) * 32, swap16(o[0], o[1]));
    }
  STAMP(p.ws, 8);
}

constexpr int TROW = 544, TCH = 53 * TROW;
constexpr int S_X = 0, XROW = 144;
constexpr int S_KV = 64 * XROW;
constexpr int S_T = S_KV + KVB;
static_assert(S_T % 16 == 0 && S_T + TCH + 64 * TROW <= LDS1, "lds");

DI f2 cmul(f2 a, f2 b) { return (f2){fmaf(a.x, b.x, -a.y * b.y), fmaf(a.x, b.y, a.y * b.x)}; }
DI f2 cexpl(float ar, float ai, float l) {
  float e = __expf(ar * l);
  float x = ai * l;
  float r = x * 0.15915494309189535f;
  float err = fmaf(x, 0.15915494309189535f, -r) + x * 6.4206383e-9f;
  float f = __builtin_amdgcn_fractf(r) + err;
  return (f2){e * __builtin_amdgcn_cosf(f), e * __builtin_amdgcn_sinf(f)};
}

DI float dpp_sum(float s) {
#define DPPADD(ctl, rm) s += __uint_as_float(__builtin_amdgcn_update_dpp(0u, __float_as_uint(s), ctl, rm, 0xf, false))
  DPPADD(0x111, 0xf); DPPADD(0x112, 0xf); DPPADD(0x114, 0xf); DPPADD(0x118, 0xf);
  DPPADD(0x142, 0xa); DPPADD(0x143, 0xc);
  return s;
}

DI void side_role(const P1& p, int b, unsigned sb) {
  const int tid = threadIdx.x, lane = tid & 63;
  const int wv = __builtin_amdgcn_readfirstlane(tid >> 6);
  const int ch = wv >> 1, n = tid & 127, kg = lane >> 4, r16 = lane & 15;
  STAMP(p.ws, 0);
  const int pi = b * 256 + tid;
  const float Are = bf2f(p.are[pi]), Aim = bf2f(p.aim[pi]), Bre = bf2f(p.bre[pi]), Bim = bf2f(p.bim[pi]);
  const float Cre = bf2f(p.cre[pi]), Cim = bf2f(p.cim[pi]);
  const float ldt = bf2f(p.ldt[2 * b + ch]);
  const int l0 = (b >> 3) * 64, c0 = (b & 7) * 64;
  u4 ut[2], gx[2], gwv;
#pragma unroll
  for (int i = 0; i < 2; ++i) ut[i] = *(const u4*)(p.u + (size_t)(l0 + i * 32 + (tid >> 3)) * D_ + c0 + (tid & 7) * 8);
  gwv = *(const u4*)(p.gw + lane * 8);
#pragma unroll
  for (int i = 0; i < 2; ++i) gx[i] = *(const u4*)(p.u + (size_t)(b * 8 + wv * 2 + i) * D_ + lane * 8);
  unsigned w2v[8];
  const int bw = min(b, 223);
#pragma unroll
  for (int i = 0; i < 8; ++i) w2v[i] = *(const unsigned*)(p.w2 + (size_t)(bw * 8 + i) * D_ + tid * 2);
  if (tid < 4) *LDSP(unsigned, sb + S_KV + tid * 4) = 0u;
  if (b == 0 && tid < 8) *(unsigned*)(p.ws + WS_CNT + tid * 128) = 0u;
  {
    const float dt = rbf(__expf(ldt));
    const float ar = dt * Are, ai = dt * Aim;
    const f2 W = cmul((f2){Cre, Cim}, (f2){dt * Bre, dt * Bim});
    const f2 e1 = cexpl(ar, ai, 1.f), e8 = cexpl(ar, ai, 8.f), e128 = cexpl(ar, ai, 128.f), e256 = cexpl(ar, ai, 256.f);
    const unsigned tb = sb + S_T + ch * TCH + n * 4;
    f2 lo = {1.f, 0.f};
#pragma unroll
    for (int i = 0; i < 16; ++i) {
      f2 hi = cmul(lo, e128);
      *LDSP(unsigned, tb + i * TROW) = pk2(lo.x, lo.y);
      *LDSP(unsigned, tb + (16 + i) * TROW) = pk2(hi.x, hi.y);
      lo = cmul(lo, e8);
    }
    f2 a[8];
    a[0] = W;
#pragma unroll
    for (int i = 1; i < 8; ++i) a[i] = cmul(a[i - 1], e1);
#define PUTA(row, v) *LDSP(unsigned, tb + (32 + (row)) * TROW) = pk2((v).x, -(v).y)
#pragma unroll
    for (int i = 0; i < 8; ++i) PUTA(i, a[i]);
#pragma unroll
    for (int i = 0; i < 4; ++i) { a[2 * i] = cmul(a[2 * i], e256); PUTA(8 + i, a[2 * i]); }
    a[0] = cmul(a[0], e256); a[4] = cmul(a[4], e256); PUTA(12, a[0]); PUTA(13, a[4]);
    a[0] = cmul(a[0], e256); a[4] = cmul(a[4], e256); PUTA(14, a[0]); PUTA(15, a[4]);
#pragma unroll
    for (int i = 0; i < 5; ++i) { a[0] = cmul(a[0], e256); PUTA(16 + i, a[0]); }
  }
  STAMP(p.ws, 1);
#pragma unroll
  for (int i = 0; i < 2; ++i) *LDSP(u4, sb + S_X + (i * 32 + (tid >> 3)) * XROW + (tid & 7) * 16) = ut[i];
  __syncthreads();
  STAMP(p.ws, 2);
  {
    f4 k0 = {0.f, 0.f, 0.f, 0.f}, k1 = k0;
    const unsigned tb = sb + S_T + ch * TCH + r16 * TROW + kg * 16;
    const unsigned lb = tb + (wv & 1) * 16 * TROW;
#pragma unroll
    for (int ks = 0; ks < 8; ++ks) {
      bf8 bl = lds128(lb + ks * 64);
      k0 = mfma16(lds128(tb + 32 * TROW + ks * 64), bl, k0);
      k1 = mfma16(lds128(tb + 48 * TROW + ks * 64), bl, k1);
    }
    const int c = (wv & 1) * 16 + r16;
    const unsigned kv = sb + S_KV + 16 + ch * 2;
    int i0 = kg < 2 ? 8 * c + 4 * kg : (kg == 2 ? 256 + 4 * c : 384 + 2 * c);
    int i1 = kg < 3 ? i0 + 2 : i0 + 64;
    unsigned pa = pk2(k0[0], k0[1]), pb = pk2(k0[2], k0[3]);
    *LDSP(us, kv + i0 * 4) = (us)pa; *LDSP(us, kv + i0 * 4 + 4) = (us)(pa >> 16);
    *LDSP(us, kv + i1 * 4) = (us)pb; *LDSP(us, kv + i1 * 4 + 4) = (us)(pb >> 16);
    pa = pk2(k1[0], k1[1]); pb = pk2(k1[2], k1[3]);
    int i2 = kg == 0 ? 512 + c : (kg == 1 && c == 0 ? 640 : 641), st = kg == 0 ? 128 : 0;
    *LDSP(us, kv + i2 * 4) = (us)pa;
    i2 = kg == 0 ? i2 : 641;
    *LDSP(us, kv + i2 * 4 + st) = (us)(pa >> 16); *LDSP(us, kv + i2 * 4 + 2 * st) = (us)pb; *LDSP(us, kv + i2 * 4 + 3 * st) = (us)(pb >> 16);
  }
  STAMP(p.ws, 3);
#pragma unroll
  for (int i = 0; i < 2; ++i) {
    unsigned a = sb + S_X + (i * 32 + kg * 8 + (r16 >> 2)) * XROW + (wv * 16 + (lane & 3) * 4) * 2;
    *(bf8*)((us*)(p.ws + WS_UT) + (size_t)(c0 + wv * 16 + r16) * L_ + l0 + i * 32 + kg * 8) = cat8(ldstr(a), ldstr(a + 4 * XROW));
  }
#pragma unroll
  for (int i = 0; i < 2; ++i) {
    float s = 0.f;
#pragma unroll
    for (int e = 0; e < 4; ++e) {
      unsigned pr = pk2(bf2f(gx[i][e] & 0xffffu) * bf2f(gwv[e] & 0xffffu), bfhi(gx[i][e]) * bfhi(gwv[e]));
      s += bf2f(pr & 0xffffu) + bfhi(pr);
    }
    s = rbf(dpp_sum(s));
    if (lane == 63) ((float*)(p.ws + WS_G))[b * 8 + wv * 2 + i] = rbf(__builtin_amdgcn_rcpf(1.f + __expf(-s)));
  }
  if (b < 224) {
    u4 lo, hi;
#pragma unroll
    for (int e = 0; e < 4; ++e) {
      lo[e] = __builtin_amdgcn_perm(w2v[2 * e + 1], w2v[2 * e], 0x05040100);
      hi[e] = __builtin_amdgcn_perm(w2v[2 * e + 1], w2v[2 * e], 0x07060302);
    }
    u4* dst = (u4*)(p.ws + WS_W2T) + (((size_t)(b >> 2) * 32 + (tid >> 3)) * 4 + (b & 3)) * 16 + (tid & 7) * 2;
    dst[0] = lo; dst[1] = hi;
  }
  __syncthreads();
  if (tid < KVB / 16) *(u4*)(p.ws + WS_KV + (size_t)b * KVB + tid * 16) = *LDSP(u4, sb + S_KV + tid * 16);
  STAMP(p.ws, 4);
}

#define STAMPEND(ws)
__global__ void __launch_bounds__(256, 2) k_stage1(P1 p) {
  __shared__ __attribute__((aligned(16))) char smem[LDS1];
  const unsigned sb = (unsigned)(uintptr_t)(__attribute__((address_space(3))) char*)smem;
  if (blockIdx.x < 224) gemm1_role(p, blockIdx.x, sb);
  else side_role(p, blockIdx.x - 224, sb);
  STAMPEND(p.ws)
}

#undef KID
#define KID 2
constexpr int C_U = 0, UPH = 1184, UCH = 4 * UPH;
constexpr int C_KV = 2 * UCH, C_K = C_KV + KVB;
constexpr int KCP = 4128, KCH = 4 * KCP;
typedef __attribute__((address_space(1))) unsigned gu32;
DI __amdgpu_buffer_rsrc_t at_rsrc(const char* ws) { return __builtin_amdgcn_make_buffer_rsrc((void*)(ws + WS_AT), 0, D_ * L_ * 2, 0x00020000); }

template <int Q0, int Q1, int Q2, int Q3>
DI void conv_wave(unsigned kb, unsigned ub, f4 (&acc)[4]) {
  constexpr int QS[4] = {Q0, Q1, Q2, Q3};
  constexpr int NT = 2 * Q0 + 2, KD = 4;
  auto rdU = [&](int rho, int s) {
    unsigned a = ub + rho * UPH + s * 64;
    return cat8(*LDSP(volatile s4, a), *LDSP(volatile s4, a + 8));
  };
  auto rdK = [&](int x) {
    unsigned a = kb + (x / NT) * KCP + (x % NT) * 256;
    return cat8(ldstr(a + 32), ldstr(a));
  };
  bf8 uf[2][NT], kf[KD];
#pragma unroll
  for (int x = 0; x < KD - 1; ++x) kf[x] = rdK(x);
#pragma unroll
  for (int s = NT - 1; s >= 0; --s) uf[0][s] = rdU(0, s);
  __builtin_amdgcn_sched_barrier(0);
#pragma unroll
  for (int rho = 0; rho < 4; ++rho)
#pragma unroll
    for (int t = 0; t < NT; ++t) {
      const int x = rho * NT + t;
      if (x + KD - 1 < 4 * NT) kf[(x + KD - 1) % KD] = rdK(x + KD - 1);
      if (rho < 3) uf[(rho + 1) & 1][t] = rdU(rho + 1, t);
#pragma unroll
      for (int i = 0; i < 4; ++i)
        if (t <= 2 * QS[i] + 1) acc[i] = mfma16(kf[x % KD], uf[rho & 1][2 * QS[i] - t + 1], acc[i]);
      __builtin_amdgcn_sched_barrier(0);
    }
}
DI void conv_role(char* ws, const us* dskp, int b, unsigned sb) {
  const int tid = threadIdx.x, lane = tid & 63;
  const int wv = __builtin_amdgcn_readfirstlane(tid >> 6);
  const int ch = wv >> 1, kg = lane >> 4, r16 = lane & 15;
  STAMP(ws, 0);
  u4 kvv = *(const u4*)(ws + WS_KV + (size_t)b * KVB + min(tid, KVB / 16 - 1) * 16);
  u4 uv[2];
#pragma unroll
  for (int c = 0; c < 2; ++c) uv[c] = *(const u4*)(ws + WS_UT + (size_t)(2 * b + c) * L_ * 2 + tid * 16);
  const float dsk = bf2f(dskp[2 * b + ch]);
  unsigned kidx[11];
#pragma unroll
  for (int e = 0; e < 11; ++e) {
    int j = 8 * tid - 3 + e;
    int ix = (int)(pk2((float)j, 0.f) & 0xffffu) - 0x4280;
    kidx[e] = sb + C_KV + 16 + (j < 128 ? j : ix) * 4;
  }
  *LDSP(unsigned, sb + C_U + (tid >> 5) * UPH + (tid & 31) * 4) = 0u;
  if (tid < 64) *LDSP(unsigned, sb + C_K + (tid >> 3) * KCP + (tid & 7) * 4) = 0u;
  *LDSP(u4, sb + C_KV + min(tid, KVB / 16 - 1) * 16) = kvv;
  __syncthreads();
  STAMP(ws, 1);
  {
    unsigned v[11];
#pragma unroll
    for (int e = 0; e < 11; ++e) v[e] = *LDSP(unsigned, kidx[e]);
#pragma unroll
    for (int c = 0; c < 2; ++c)
#pragma unroll
      for (int r = 0; r < 4; ++r)
        *LDSP(unsigned, sb + C_U + c * UCH + r * UPH + 128 + tid * 4) =
            __builtin_amdgcn_perm(uv[c][2 + (r >> 1)], uv[c][r >> 1], (r & 1) ? 0x07060302 : 0x05040100);
#pragma unroll
    for (int c = 0; c < 2; ++c) {
      unsigned pr[10];
#pragma unroll
      for (int i = 0; i < 10; ++i) pr[i] = __builtin_amdgcn_perm(v[i + 1], v[i], c ? 0x07060302 : 0x05040100);
      unsigned a = sb + C_K + c * KCH + 32 + tid * 16;
#pragma unroll
      for (int r = 0; r < 4; ++r) *LDSP(u4, a + r * KCP) = (u4){pr[3 - r], pr[5 - r], pr[7 - r], pr[9 - r]};
    }
  }
  __syncthreads();
  STAMP(ws, 2);
  f4 acc[4];
#pragma unroll
  for (int i = 0; i < 4; ++i) acc[i] = (f4){0.f, 0.f, 0.f, 0.f};
  const unsigned kb = sb + C_K + ch * KCH + (16 + 112 - 4 * (8 * kg + 4 + (r16 >> 2)) + 4 * (lane & 3)) * 2;
  const unsigned ub = sb + C_U + ch * UCH + (64 - 32 - 28 + 4 * r16 + 8 * kg) * 2;
  int QS[4];
  if (wv & 1) { conv_wave<6, 5, 3, 0>(kb, ub, acc); QS[0] = 6; QS[1] = 5; QS[2] = 3; QS[3] = 0; }
  else { conv_wave<7, 4, 2, 1>(kb, ub, acc); QS[0] = 7; QS[1] = 4; QS[2] = 2; QS[3] = 1; }
  STAMP(ws, 3);
  u2 o[4];
#pragma unroll
  for (int i = 0; i < 4; ++i) {
    float y[4];
#pragma unroll
    for (int r = 0; r < 4; ++r) {
      float uu = bf2f(*LDSP(us, sb + C_U + ch * UCH + r * UPH + (64 + 64 * QS[i] + 4 * r16 + kg) * 2));
      unsigned pr = pk2(acc[i][r], uu * dsk);
      y[r] = bf2f(pr & 0xffffu) + bfhi(pr);
    }
    o[i] = (u2){pk2(y[0], y[1]), pk2(y[2], y[3])};
  }
  const auto rs = at_rsrc(ws);
#pragma unroll
  for (int i = 0; i < 2; ++i) {
    int q = (kg & 1) ? QS[2 * i + 1] : QS[2 * i];
    __builtin_amdgcn_raw_buffer_store_b128(swap16(o[2 * i], o[2 * i + 1]), rs, ((2 * b + ch) * L_ + 256 * q + 16 * r16 + 8 * (kg >> 1)) * 2, 0, 16);
  }
  WAITVM(0);
  __syncthreads();
  if (tid == 0) __hip_atomic_fetch_add((gu32*)(ws + WS_CNT + (b >> 5) * 128), 1u, __ATOMIC_RELAXED, __HIP_MEMORY_SCOPE_AGENT);
  STAMP(ws, 4);
}

constexpr int R_ROW = 272, R_TILE = 16 * R_ROW;
constexpr int G2_A = 0, G2_R = 8192, LDS2 = G2_R + 16 * R_TILE;
static_assert(C_K % 16 == 0 && C_K + 2 * KCH <= LDS2 && LDS2 <= 81920, "lds");
#define G2D 4

DI void gemm2_role(const char* ws, us* out, int gb, unsigned sb) {
  const int tid = threadIdx.x, lane = tid & 63, kg = lane >> 4, r16 = lane & 15;
  const int wv = __builtin_amdgcn_readfirstlane(tid >> 6);
  const int xcd = gb & 7, slot = gb >> 3;
  const int mt = 4 * xcd + (slot & 3), nt = slot >> 2, m0 = mt * 64, n0 = nt * 64;
  const bf8* hp = (const bf8*)(ws + WS_H) + ((size_t)wv * 14 * 128 + mt * 4) * 64 + lane;
  const bf8* wp = (const bf8*)(ws + WS_W2T) + ((size_t)wv * 14 * 32 + nt * 4) * 64 + lane;
  STAMP(ws, 0);
  bf8 hf[14][4], wf[14][4];
  auto ld = [&](int s) {
#pragma unroll
    for (int i = 0; i < 4; ++i) { wf[s][i] = wp[(s * 32 + i) * 64]; hf[s][i] = hp[(s * 128 + i) * 64]; }
  };
#pragma unroll
  for (int s = 0; s < G2D; ++s) ld(s);
  const float g = ((const float*)(ws + WS_G))[m0 + wv * 16 + r16];
  f4 acc[4][4];
#pragma unroll
  for (int i = 0; i < 4; ++i)
#pragma unroll
    for (int j = 0; j < 4; ++j) acc[i][j] = (f4){0.f, 0.f, 0.f, 0.f};
  gu32* cnt = (gu32*)(ws + WS_CNT + nt * 128);
  unsigned seen = 0;
#pragma unroll
  for (int s = 0; s < 14; ++s) {
    __builtin_amdgcn_sched_barrier(0);
#pragma unroll
    for (int i = 0; i < 4; ++i)
#pragma unroll
      for (int j = 0; j < 4; ++j) acc[i][j] = mfma16(wf[s][i], hf[s][j], acc[i][j]);
    __builtin_amdgcn_sched_barrier(0);
    if (s == 0 || s == 13) STAMP(ws, 1 + s / 13);
    if (s + G2D < 14) ld(s + G2D);
    if (s == 11) seen = __hip_atomic_load(cnt, __ATOMIC_RELAXED, __HIP_MEMORY_SCOPE_AGENT);
  }
#pragma unroll
  for (int i = 0; i < 4; ++i)
#pragma unroll
    for (int j = 0; j < 4; ++j) asm volatile("" : "+v"(acc[i][j]));
  for (unsigned spin = 0; __builtin_amdgcn_readfirstlane(seen) < 32u && spin < (1u << 24); ++spin) {
    __builtin_amdgcn_s_sleep(2);
    seen = __hip_atomic_load(cnt, __ATOMIC_RELAXED, __HIP_MEMORY_SCOPE_AGENT);
  }
  __builtin_amdgcn_fence(__ATOMIC_ACQUIRE, "wavefront");
  STAMP(ws, 3);
  const auto rs = at_rsrc(ws);
  u4 av[2];
#pragma unroll
  for (int i = 0; i < 2; ++i)
    av[i] = __builtin_amdgcn_raw_buffer_load_b128(rs, ((n0 + i * 32 + (tid >> 3)) * L_ + m0 + (tid & 7) * 8) * 2, 0, 16);
#pragma unroll
  for (int j = 0; j < 4; ++j)
#pragma unroll
    for (int i = 0; i < 4; ++i)
      *LDSP(f4, sb + G2_R + (j * 4 + wv) * R_TILE + r16 * R_ROW + (16 * i + 4 * kg) * 4) = acc[i][j];
#pragma unroll
  for (int i = 0; i < 2; ++i) *LDSP(u4, sb + G2_A + (i * 32 + (tid >> 3)) * 128 + (tid & 7) * 16) = av[i];
  __syncthreads();
  STAMP(ws, 4);
  const float omg = rbf(1.f - g);
  f4 ps[4][4];
  s4 at[4];
#pragma unroll
  for (int i = 0; i < 4; ++i) {
#pragma unroll
    for (int w = 0; w < 4; ++w) ps[i][w] = *LDSP(f4, sb + G2_R + (wv * 4 + w) * R_TILE + r16 * R_ROW + (16 * i + 4 * kg) * 4);
    at[i] = ldstr(sb + G2_A + (16 * i + 4 * kg + (r16 >> 2)) * 128 + (wv * 16 + 4 * (lane & 3)) * 2);
  }
  __builtin_amdgcn_sched_barrier(0);
  u2 o[4];
#pragma unroll
  for (int i = 0; i < 4; ++i) {
    f4 s = (ps[i][0] + ps[i][1]) + (ps[i][2] + ps[i][3]);
    float y[4];
#pragma unroll
    for (int r = 0; r < 4; ++r) {
      unsigned pr = pk2(g * bf2f((us)at[i][r]), omg * rbf(s[r]));
      y[r] = bf2f(pr & 0xffffu) + bfhi(pr);
    }
    o[i] = (u2){pk2(y[0], y[1]), pk2(y[2], y[3])};
  }
#pragma unroll
  for (int i = 0; i < 2; ++i)
    *(u4*)(out + (size_t)(m0 + wv * 16 + r16) * D_ + n0 + 32 * i + 16 * (kg & 1) + 8 * (kg >> 1)) = swap16(o[2 * i], o[2 * i + 1]);
  STAMP(ws, 5);
}

__global__ void __launch_bounds__(256, 2) k_stage2(char* ws, const us* dsk, us* out) {
  __shared__ __attribute__((aligned(16))) char smem[LDS2];
  const unsigned sb = (unsigned)(uintptr_t)(__attribute__((address_space(3))) char*)smem;
  if (blockIdx.x < 256) conv_role(ws, dsk, blockIdx.x, sb);
  else gemm2_role(ws, out, blockIdx.x - 256, sb);
  STAMPEND(ws)
}

extern "C" void kernel_launch(void* const* d_in, const int* in_sizes, int n_in, void* d_out, int out_size,
                              void* d_ws, size_t ws_size, hipStream_t stream) {
  P1 p;
  p.u = (const us*)d_in[0]; p.are = (const us*)d_in[1]; p.aim = (const us*)d_in[2];
  p.bre = (const us*)d_in[3]; p.bim = (const us*)d_in[4]; p.cre = (const us*)d_in[5]; p.cim = (const us*)d_in[6];
  p.ldt = (const us*)d_in[7];
  p.w1 = (const us*)d_in[10]; p.w3 = (const us*)d_in[11]; p.w2 = (const us*)d_in[12]; p.gw = (const us*)d_in[13];
  p.ws = (char*)d_ws;
  k_stage1<<<480, 256, 0, stream>>>(p);
  k_stage2<<<512, 256, 0, stream>>>((char*)d_ws, (const us*)d_in[8], (us*)d_out);
}
```

```cpp
#include <hip/hip_runtime.h>
#include <hip/hip_bf16.h>
#include <stdint.h>

typedef __bf16 bf8 __attribute__((ext_vector_type(8)));
typedef __bf16 bf2 __attribute__((ext_vector_type(2)));
typedef short s4 __attribute__((ext_vector_type(4)));
typedef short s8 __attribute__((ext_vector_type(8)));
typedef float f2 __attribute__((ext_vector_type(2)));
typedef float f4 __attribute__((ext_vector_type(4)));
typedef float f16v __attribute__((ext_vector_type(16)));
typedef unsigned u2 __attribute__((ext_vector_type(2)));
typedef unsigned u4 __attribute__((ext_vector_type(4)));
typedef unsigned short us;
#define DI __device__ __forceinline__
#define LDSP(T, off) ((__attribute__((address_space(3))) T*)(uintptr_t)(off))

constexpr int L_ = 2048, D_ = 512, F_ = 1792;
constexpr int KVB = 2624;
constexpr size_t WS_CNT = 0;
constexpr size_t WS_H = 4096;
constexpr size_t WS_W2T = WS_H + (size_t)F_ * L_ * 2;
constexpr size_t WS_AT = WS_W2T + (size_t)F_ * D_ * 2;
constexpr size_t WS_UT = WS_AT + (size_t)D_ * L_ * 2;
constexpr size_t WS_G = WS_UT + (size_t)D_ * L_ * 2;
constexpr size_t WS_KV = WS_G + (size_t)L_ * 4;

DI float bf2f(unsigned v) { return __uint_as_float(v << 16); }
DI float bfhi(unsigned v) { return __uint_as_float(v & 0xffff0000u); }
DI unsigned pk2(float a, float b) { f2 v = {a, b}; return __builtin_bit_cast(unsigned, __builtin_convertvector(v, bf2)); }
DI float rbf(float a) { return bf2f(pk2(a, a) & 0xffffu); }
DI f4 mfma16(bf8 a, bf8 b, f4 c) { return __builtin_amdgcn_mfma_f32_16x16x32_bf16(a, b, c, 0, 0, 0); }
DI f16v mfma32(bf8 a, bf8 b, f16v c) { return __builtin_amdgcn_mfma_f32_32x32x16_bf16(a, b, c, 0, 0, 0); }
DI bf8 lds128(unsigned off) { return *LDSP(bf8, off); }
DI bf8 cat8(s4 a, s4 b) { return __builtin_bit_cast(bf8, __builtin_shufflevector(a, b, 0, 1, 2, 3, 4, 5, 6, 7)); }
DI s4 ldstr(unsigned off) { return __builtin_amdgcn_ds_read_tr16_b64_v4i16(LDSP(s4, off)); }
DI void glds16(unsigned voff, const void* sbase, unsigned dst) {
  asm volatile("s_mov_b32 m0, %2\n\ts_nop 0\n\tglobal_load_lds_dwordx4 %0, %1" :: "v"(voff), "s"(sbase), "s"(dst) : "memory");
}
#define STH(ptr, v) (*(ptr) = (v))
#define WAITVM(n) asm volatile("s_waitcnt vmcnt(" #n ")" ::: "memory")
DI void bar() { asm volatile("s_waitcnt lgkmcnt(0)\n\ts_barrier" ::: "memory"); }

#define STAMP(ws, i) do {} while (0)
#define KID 1
#define CPF 1
template <int LINES> DI unsigned code_touch() {
  unsigned long long pc = __builtin_amdgcn_s_getpc();
  unsigned v = *(__attribute__((address_space(1))) unsigned*)((pc & ~127ull) + min((int)threadIdx.x, LINES - 1) * 128ull);
  asm volatile("" ::: "memory");
  return v;
}
struct P1 {
  const us *u, *are, *aim, *bre, *bim, *cre, *cim, *ldt, *w1, *w3, *w2, *gw;
  char* ws;
};
DI u4 swap16(u2 a, u2 b) {
  auto x = __builtin_amdgcn_permlane16_swap(a[0], b[0], false, false);
  auto y = __builtin_amdgcn_permlane16_swap(a[1], b[1], false, false);
  return (u4){x[0], y[0], x[1], y[1]};
}

DI u4 swap32(u2 a, u2 b) {
  auto x = __builtin_amdgcn_permlane32_swap(a[0], b[0], false, false);
  auto y = __builtin_amdgcn_permlane32_swap(a[1], b[1], false, false);
  return (u4){x[0], y[0], x[1], y[1]};
}
template <int V> struct IC { static constexpr int v = V; };

constexpr int XS = 16384, WSZ = 16384;
constexpr int G1_X = 0, G1_W = 2 * XS;
constexpr int LDS1 = 81920;
#define G1MAP 1
#define ROT 3

DI void gemm1_role(const P1& p, int gb, unsigned sb) {
  const int tid = threadIdx.x, lane = tid & 63;
  const int wv = __builtin_amdgcn_readfirstlane(tid >> 6);
  const int xcd = gb & 7, slot = gb >> 3;
  const int mt = 2 * xcd + (slot & 1), nt = slot >> 1;
  const int m0 = mt * 128, n0 = nt * 128;
  const int rot = ROT == 1 ? (slot & 7) : (ROT == 2 ? ((slot >> 1) & 7) : (ROT == 3 ? xcd : 0));
  STAMP(p.ws, 0);
  unsigned xoff[2];
#pragma unroll
  for (int i = 0; i < 2; ++i) xoff[i] = (lane >> 3) * (D_ * 2) + (((lane & 7) ^ (4 * i + (lane >> 4))) << 4);
  const us* xs = p.u + (size_t)(m0 + wv * 32) * D_;
  auto issueX1 = [&](int t, int i) { glds16(xoff[i & 1], xs + ((t + rot) & 7) * 64 + i * 8 * D_, sb + G1_X + (t & 1) * XS + wv * 4096 + i * 1024); };
#pragma unroll
  for (int i = 0; i < 4; ++i) issueX1(0, i);
  const unsigned woff = (lane >> 4) * (F_ * 2) + (((lane & 15) ^ ((lane >> 4) << 2)) << 4);
  const us* ws1 = p.w1 + wv * 8 * F_ + n0;
  const us* ws3 = p.w3 + wv * 8 * F_ + n0;
  auto issueW1 = [&](int s, int slot3, int i) {
    const int ko = (((s + 2 * rot) & 15) * 32 + (i & 1) * 4) * F_;
    glds16(woff, ((i >> 1) ? ws3 : ws1) + ko, sb + G1_W + slot3 + (i >> 1) * 8192 + wv * 2048 + (i & 1) * 1024);
  };
#pragma unroll
  for (int i = 0; i < 4; ++i) issueW1(0, 0, i);
#pragma unroll
  for (int i = 0; i < 4; ++i) issueW1(1, WSZ, i);
#pragma unroll
  for (int i = 0; i < 4; ++i) issueX1(1, i);
#pragma unroll
  for (int i = 0; i < 4; ++i) issueW1(2, 2 * WSZ, i);

  const int wm = wv & 1, wn = wv >> 1, hi = lane >> 5, r32 = lane & 31, q = (lane >> 2) & 3;
  unsigned xb[4], wb[2];
#pragma unroll
  for (int c = 0; c < 4; ++c) xb[c] = sb + G1_X + (wm * 64 + r32) * 128 + (((c * 2 + hi) ^ ((r32 >> 1) & 7)) << 4);
#pragma unroll
  for (int j = 0; j < 2; ++j) wb[j] = sb + G1_W + (8 * hi + q) * 256 + (((wn * 2 + j) ^ q) << 6) + (lane & 16) * 2 + (lane & 3) * 8;
  auto rdW = [&](int so, int f) {
    unsigned a = wb[(f >> 1) & 1] + so + (f & 1) * 8192 + (f >> 2) * 4096;
    return cat8(ldstr(a), ldstr(a + 1024));
  };
  f16v acc[4][2];
#pragma unroll
  for (int j = 0; j < 4; ++j)
#pragma unroll
    for (int i = 0; i < 2; ++i)
#pragma unroll
      for (int r = 0; r < 16; ++r) acc[j][i][r] = 0.f;
  bf8 xf[2][4], wf[2][8];
  WAITVM(12);
  bar();
  STAMP(p.ws, 1);
#pragma unroll
  for (int i = 0; i < 4; ++i) xf[0][i] = lds128(xb[i >> 1] + (i & 1) * 4096);
#pragma unroll
  for (int f = 0; f < 8; ++f) wf[0][f] = rdW(0, f);
  int cur = 0;
  auto body = [&](int it, auto NG) {
#pragma unroll
    for (int h = 0; h < 2; ++h) {
      const int s = 2 * it + h;
      const bool go = h < NG.v, last = NG.v == 0 && h;
      if (!last) {
        if (h) WAITVM(4);
        else if (NG.v) WAITVM(8);
        else WAITVM(0);
        bar();
      }
      const int nxt = cur == 2 * WSZ ? 0 : cur + WSZ;
      if (h) { xb[0] ^= XS; xb[1] ^= XS; }
      __builtin_amdgcn_sched_barrier(0);
#pragma unroll
      for (int f = 0; f < 8; ++f) {
        acc[f & 3][0] = mfma32(wf[h][f], xf[h][(f >> 2) * 2], acc[f & 3][0]);
        __builtin_amdgcn_sched_barrier(0);
        if (!last) {
          if (f < 4) xf[h ^ 1][f] = lds128(xb[(h ^ 1) * 2 + (f >> 1)] + (f & 1) * 4096);
          wf[h ^ 1][f] = rdW(nxt, f);
        }
        __builtin_amdgcn_sched_barrier(0);
        acc[f & 3][1] = mfma32(wf[h][f], xf[h][(f >> 2) * 2 + 1], acc[f & 3][1]);
        __builtin_amdgcn_sched_barrier(0);
        if (go) {
          if (h) { if (f < 4) issueW1(s + 3, cur, f); else issueX1(it + 2, f - 4); }
          else if (!(f & 1)) issueW1(s + 3, cur, f >> 1);
        }
        __builtin_amdgcn_sched_barrier(0);
      }
      if (!h) { xb[2] ^= XS; xb[3] ^= XS; }
      cur = nxt;
    }
  };
#pragma unroll 1
  for (int it = 0; it < 6; ++it) body(it, IC<2>());
  body(6, IC<1>());
  body(7, IC<0>());
  STAMP(p.ws, 7);
  u4* hws = (u4*)(p.ws + WS_H) + ((size_t)(nt * 8 + wn * 4) * 64 + mt * 4 + wm * 2) * 64 + lane;
#pragma unroll
  for (int j = 0; j < 2; ++j)
#pragma unroll
    for (int i = 0; i < 2; ++i)
#pragma unroll
      for (int c = 0; c < 2; ++c) {
        u2 o[2];
#pragma unroll
        for (int r = 0; r < 4; ++r) {
          f2 g = {acc[2 * j][i][8 * c + 2 * r], acc[2 * j][i][8 * c + 2 * r + 1]};
          f2 up = {acc[2 * j + 1][i][8 * c + 2 * r], acc[2 * j + 1][i][8 * c + 2 * r + 1]};
          f2 e = g * -1.4426950408889634f;
          e = (f2){__builtin_amdgcn_exp2f(e.x), __builtin_amdgcn_exp2f(e.y)} + 1.f;
          e = (g * (f2){__builtin_amdgcn_rcpf(e.x), __builtin_amdgcn_rcpf(e.y)}) * up;
          o[r >> 1][r & 1] = pk2(e.x, e.y);
        }
        STH(hws + ((size_t)(2 * j + c) * 64 + i) * 64, swap32(o[0], o[1]));
      }
  STAMP(p.ws, 8);
}

constexpr int TROW = 544, TCH = 53 * TROW;
constexpr int S_X = 0, XROW = 144;
constexpr int S_KV = 64 * XROW;
constexpr int S_T = S_KV + KVB;
static_assert(S_T % 16 == 0 && S_T + TCH + 64 * TROW <= LDS1, "lds");

DI f2 cmul(f2 a, f2 b) { return (f2){fmaf(a.x, b.x, -a.y * b.y), fmaf(a.x, b.y, a.y * b.x)}; }
DI f2 cexpl(float ar, float ai, float l) {
  float e = __expf(ar * l);
  float x = ai * l;
  float r = x * 0.15915494309189535f;
  float err = fmaf(x, 0.15915494309189535f, -r) + x * 6.4206383e-9f;
  float f = __builtin_amdgcn_fractf(r) + err;
  return (f2){e * __builtin_amdgcn_cosf(f), e * __builtin_amdgcn_sinf(f)};
}

DI float dpp_sum(float s) {
#define DPPADD(ctl, rm) s += __uint_as_float(__builtin_amdgcn_update_dpp(0u, __float_as_uint(s), ctl, rm, 0xf, false))
  DPPADD(0x111, 0xf); DPPADD(0x112, 0xf); DPPADD(0x114, 0xf); DPPADD(0x118, 0xf);
  DPPADD(0x142, 0xa); DPPADD(0x143, 0xc);
  return s;
}

DI void side_role(const P1& p, int b, unsigned sb) {
  const int tid = threadIdx.x, lane = tid & 63;
  const int wv = __builtin_amdgcn_readfirstlane(tid >> 6);
  const int ch = wv >> 1, n = tid & 127, kg = lane >> 4, r16 = lane & 15;
  STAMP(p.ws, 0);
  const int pi = b * 256 + tid;
  const float Are = bf2f(p.are[pi]), Aim = bf2f(p.aim[pi]), Bre = bf2f(p.bre[pi]), Bim = bf2f(p.bim[pi]);
  const float Cre = bf2f(p.cre[pi]), Cim = bf2f(p.cim[pi]);
  const float ldt = bf2f(p.ldt[2 * b + ch]);
  const int l0 = (b >> 3) * 64, c0 = (b & 7) * 64;
  u4 ut[2], gx[2], gwv;
#pragma unroll
  for (int i = 0; i < 2; ++i) ut[i] = *(const u4*)(p.u + (size_t)(l0 + i * 32 + (tid >> 3)) * D_ + c0 + (tid & 7) * 8);
  gwv = *(const u4*)(p.gw + lane * 8);
#pragma unroll
  for (int i = 0; i < 2; ++i) gx[i] = *(const u4*)(p.u + (size_t)(b * 8 + wv * 2 + i) * D_ + lane * 8);
  unsigned w2v[8];
  const int bw = min(b, 223);
#pragma unroll
  for (int i = 0; i < 8; ++i) w2v[i] = *(const unsigned*)(p.w2 + (size_t)(bw * 8 + i) * D_ + tid * 2);
  if (tid < 4) *LDSP(unsigned, sb + S_KV + tid * 4) = 0u;
  if (b == 0 && tid < 8) *(unsigned*)(p.ws + WS_CNT + tid * 128) = 0u;
  {
    const float dt = rbf(__expf(ldt));
    const float ar = dt * Are, ai = dt * Aim;
    const f2 W = cmul((f2){Cre, Cim}, (f2){dt * Bre, dt * Bim});
    const f2 e1 = cexpl(ar, ai, 1.f), e8 = cexpl(ar, ai, 8.f), e128 = cexpl(ar, ai, 128.f), e256 = cexpl(ar, ai, 256.f);
    const unsigned tb = sb + S_T + ch * TCH + n * 4;
    f2 lo = {1.f, 0.f};
#pragma unroll 2
    for (int i = 0; i < 16; ++i) {
      f2 hi = cmul(lo, e128);
      *LDSP(unsigned, tb + i * TROW) = pk2(lo.x, lo.y);
      *LDSP(unsigned, tb + (16 + i) * TROW) = pk2(hi.x, hi.y);
      lo = cmul(lo, e8);
    }
    f2 a[8];
    a[0] = W;
#pragma unroll
    for (int i = 1; i < 8; ++i) a[i] = cmul(a[i - 1], e1);
#define PUTA(row, v) *LDSP(unsigned, tb + (32 + (row)) * TROW) = pk2((v).x, -(v).y)
#pragma unroll
    for (int i = 0; i < 8; ++i) PUTA(i, a[i]);
#pragma unroll
    for (int i = 0; i < 4; ++i) { a[2 * i] = cmul(a[2 * i], e256); PUTA(8 + i, a[2 * i]); }
    a[0] = cmul(a[0], e256); a[4] = cmul(a[4], e256); PUTA(12, a[0]); PUTA(13, a[4]);
    a[0] = cmul(a[0], e256); a[4] = cmul(a[4], e256); PUTA(14, a[0]); PUTA(15, a[4]);
#pragma unroll
    for (int i = 0; i < 5; ++i) { a[0] = cmul(a[0], e256); PUTA(16 + i, a[0]); }
  }
  STAMP(p.ws, 1);
#pragma unroll
  for (int i = 0; i < 2; ++i) *LDSP(u4, sb + S_X + (i * 32 + (tid >> 3)) * XROW + (tid & 7) * 16) = ut[i];
  __syncthreads();
  STAMP(p.ws, 2);
  {
    f4 k0 = {0.f, 0.f, 0.f, 0.f}, k1 = k0;
    const unsigned tb = sb + S_T + ch * TCH + r16 * TROW + kg * 16;
    const unsigned lb = tb + (wv & 1) * 16 * TROW;
#pragma unroll
    for (int ks = 0; ks < 8; ++ks) {
      bf8 bl = lds128(lb + ks * 64);
      k0 = mfma16(lds128(tb + 32 * TROW + ks * 64), bl, k0);
      k1 = mfma16(lds128(tb + 48 * TROW + ks * 64), bl, k1);
    }
    const int c = (wv & 1) * 16 + r16;
    const unsigned kv = sb + S_KV + 16 + ch * 2;
    int i0 = kg < 2 ? 8 * c + 4 * kg : (kg == 2 ? 256 + 4 * c : 384 + 2 * c);
    int i1 = kg < 3 ? i0 + 2 : i0 + 64;
    unsigned pa = pk2(k0[0], k0[1]), pb = pk2(k0[2], k0[3]);
    *LDSP(us, kv + i0 * 4) = (us)pa; *LDSP(us, kv + i0 * 4 + 4) = (us)(pa >> 16);
    *LDSP(us, kv + i1 * 4) = (us)pb; *LDSP(us, kv + i1 * 4 + 4) = (us)(pb >> 16);
    pa = pk2(k1[0], k1[1]); pb = pk2(k1[2], k1[3]);
    int i2 = kg == 0 ? 512 + c : (kg == 1 && c == 0 ? 640 : 641), st = kg == 0 ? 128 : 0;
    *LDSP(us, kv + i2 * 4) = (us)pa;
    i2 = kg == 0 ? i2 : 641;
    *LDSP(us, kv + i2 * 4 + st) = (us)(pa >> 16); *LDSP(us, kv + i2 * 4 + 2 * st) = (us)pb; *LDSP(us, kv + i2 * 4 + 3 * st) = (us)(pb >> 16);
  }
  STAMP(p.ws, 3);
#pragma unroll
  for (int i = 0; i < 2; ++i) {
    unsigned a = sb + S_X + (i * 32 + kg * 8 + (r16 >> 2)) * XROW + (wv * 16 + (lane & 3) * 4) * 2;
    *(bf8*)((us*)(p.ws + WS_UT) + (size_t)(c0 + wv * 16 + r16) * L_ + l0 + i * 32 + kg * 8) = cat8(ldstr(a), ldstr(a + 4 * XROW));
  }
#pragma unroll
  for (int i = 0; i < 2; ++i) {
    float s = 0.f;
#pragma unroll
    for (int e = 0; e < 4; ++e) {
      unsigned pr = pk2(bf2f(gx[i][e] & 0xffffu) * bf2f(gwv[e] & 0xffffu), bfhi(gx[i][e]) * bfhi(gwv[e]));
      s += bf2f(pr & 0xffffu) + bfhi(pr);
    }
    s = rbf(dpp_sum(s));
    if (lane == 63) ((float*)(p.ws + WS_G))[b * 8 + wv * 2 + i] = rbf(__builtin_amdgcn_rcpf(1.f + __expf(-s)));
  }
  if (b < 224) {
    u4 lo, hi;
#pragma unroll
    for (int e = 0; e < 4; ++e) {
      lo[e] = __builtin_amdgcn_perm(w2v[2 * e + 1], w2v[2 * e], 0x05040100);
      hi[e] = __builtin_amdgcn_perm(w2v[2 * e + 1], w2v[2 * e], 0x07060302);
    }
    u4* dst = (u4*)(p.ws + WS_W2T) + (((size_t)(b >> 1) * 16 + (tid >> 4)) * 2 + (b & 1)) * 32 + (tid & 15) * 2;
    dst[0] = lo; dst[1] = hi;
  }
  __syncthreads();
  if (tid < KVB / 16) *(u4*)(p.ws + WS_KV + (size_t)b * KVB + tid * 16) = *LDSP(u4, sb + S_KV + tid * 16);
  STAMP(p.ws, 4);
}

#define STAMPEND(ws)
__global__ void __launch_bounds__(256, 2) k_stage1(P1 p) {
  __shared__ __attribute__((aligned(16))) char smem[LDS1];
  const unsigned sb = (unsigned)(uintptr_t)(__attribute__((address_space(3))) char*)smem;
  const unsigned ct = code_touch<80>();
  if (blockIdx.x < 224) gemm1_role(p, blockIdx.x, sb);
  else side_role(p, blockIdx.x - 224, sb);
  asm volatile("" :: "v"(ct));
  STAMPEND(p.ws)
}

#undef KID
#define KID 2
constexpr int C_U = 0, UPH = 1184, UCH = 4 * UPH;
constexpr int C_KV = 2 * UCH, C_K = C_KV + KVB;
constexpr int KCP = 4128, KCH = 4 * KCP, C_X = C_K + 2 * KCH + 1024;
typedef __attribute__((address_space(1))) unsigned gu32;
DI __amdgpu_buffer_rsrc_t at_rsrc(const char* ws) { return __builtin_amdgcn_make_buffer_rsrc((void*)(ws + WS_AT), 0, D_ * L_ * 2, 0x00020000); }

DI void conv_role(char* ws, const us* dskp, int b, unsigned sb) {
  const int tid = threadIdx.x, lane = tid & 63;
  const int wv = __builtin_amdgcn_readfirstlane(tid >> 6);
  const int ch = wv >> 1, kg = lane >> 4, r16 = lane & 15;
  STAMP(ws, 0);
#define CPRIO 3
  __builtin_amdgcn_s_setprio(CPRIO);
  u4 kvv = *(const u4*)(ws + WS_KV + (size_t)b * KVB + min(tid, KVB / 16 - 1) * 16);
  u4 uv[2];
#pragma unroll
  for (int c = 0; c < 2; ++c) uv[c] = *(const u4*)(ws + WS_UT + (size_t)(2 * b + c) * L_ * 2 + tid * 16);
  const float dsk = bf2f(dskp[2 * b + ch]);
  unsigned kidx[11];
#pragma unroll
  for (int e = 0; e < 11; ++e) {
    int j = 8 * tid - 3 + e;
    int ix = (int)(pk2((float)j, 0.f) & 0xffffu) - 0x4280;
    kidx[e] = sb + C_KV + 16 + (j < 128 ? j : ix) * 4;
  }
  *LDSP(unsigned, sb + C_U + (tid >> 5) * UPH + (tid & 31) * 4) = 0u;
  if (tid < 64) *LDSP(unsigned, sb + C_K + (tid >> 3) * KCP + (tid & 7) * 4) = 0u;
  *LDSP(u4, sb + C_KV + min(tid, KVB / 16 - 1) * 16) = kvv;
  __syncthreads();
  STAMP(ws, 1);
  {
    unsigned v[11];
#pragma unroll
    for (int e = 0; e < 11; ++e) v[e] = *LDSP(unsigned, kidx[e]);
#pragma unroll
    for (int c = 0; c < 2; ++c)
#pragma unroll
      for (int r = 0; r < 4; ++r)
        *LDSP(unsigned, sb + C_U + c * UCH + r * UPH + 128 + tid * 4) =
            __builtin_amdgcn_perm(uv[c][2 + (r >> 1)], uv[c][r >> 1], (r & 1) ? 0x07060302 : 0x05040100);
#pragma unroll
    for (int c = 0; c < 2; ++c) {
      unsigned pr[10];
#pragma unroll
      for (int i = 0; i < 10; ++i) pr[i] = __builtin_amdgcn_perm(v[i + 1], v[i], c ? 0x07060302 : 0x05040100);
      unsigned a = sb + C_K + c * KCH + 32 + tid * 16;
#pragma unroll
      for (int r = 0; r < 4; ++r) *LDSP(u4, a + r * KCP) = (u4){pr[3 - r], pr[5 - r], pr[7 - r], pr[9 - r]};
    }
  }
  __syncthreads();
  STAMP(ws, 2);
  f4 acc[8];
#pragma unroll
  for (int i = 0; i < 8; ++i) acc[i] = (f4){0.f, 0.f, 0.f, 0.f};
  const int hr = wv & 1;
  unsigned kb = sb + C_K + ch * KCH + 2 * hr * KCP + (16 + 112 - 4 * (8 * kg + 4 + (r16 >> 2)) + 4 * (lane & 3)) * 2;
  unsigned ub = sb + C_U + ch * UCH + 2 * hr * UPH + (64 - 32 - 28 + 4 * r16 + 8 * kg) * 2;
  auto rdU = [&](int o) { return cat8(*LDSP(volatile s4, ub + o), *LDSP(volatile s4, ub + o + 8)); };
  auto rdK = [&](int o) { return cat8(ldstr(kb + o + 32), ldstr(kb + o)); };
  bf8 uf[16], kf[4];
#pragma unroll
  for (int x = 0; x < 3; ++x) kf[x] = rdK(x * 256);
#pragma unroll
  for (int s = 15; s >= 0; s -= 2) uf[s] = rdU(s * 64);
#pragma unroll
  for (int s = 14; s >= 0; s -= 2) uf[s] = rdU(s * 64);
  __builtin_amdgcn_sched_barrier(0);
#pragma unroll 1
  for (int rho = 0; rho < 2; ++rho) {
#pragma unroll
    for (int t = 0; t < 16; ++t) {
      kf[(t + 3) & 3] = rdK(t < 13 ? (t + 3) * 256 : KCP + (t - 13) * 256);
#pragma unroll
      for (int q = 7; q >= 0; --q)
        if (t <= 2 * q + 1) acc[q] = mfma16(kf[t & 3], uf[2 * q - t + 1], acc[q]);
      uf[15 - t] = rdU(UPH + (15 - t) * 64);
      __builtin_amdgcn_sched_barrier(0);
    }
    kb += KCP; ub += UPH;
  }
  STAMP(ws, 3);
  const unsigned xb = sb + C_X + ch * 8192 + lane * 16;
#pragma unroll
  for (int i = 0; i < 4; ++i) {
    f4 lo = acc[i], hi = acc[4 + i];
    asm volatile("" : "+v"(lo), "+v"(hi));
    *LDSP(f4, xb + (hr * 4 + i) * 1024) = hr ? lo : hi;
    acc[i] = hr ? hi : lo;
  }
  float uu[4][4];
#pragma unroll
  for (int i = 0; i < 4; ++i)
#pragma unroll
    for (int r = 0; r < 4; ++r) uu[i][r] = dsk * bf2f(*LDSP(us, sb + C_U + ch * UCH + r * UPH + (64 + 64 * (4 * hr + i) + 4 * r16 + kg) * 2));
  __syncthreads();
  u2 o[4];
#pragma unroll
  for (int i = 0; i < 4; ++i) {
    f4 y = *LDSP(f4, xb + ((hr ^ 1) * 4 + i) * 1024) + acc[i];
    o[i] = (u2){pk2(y[0] + uu[i][0], y[1] + uu[i][1]), pk2(y[2] + uu[i][2], y[3] + uu[i][3])};
  }
  const auto rs = at_rsrc(ws);
#pragma unroll
  for (int i = 0; i < 2; ++i) {
    int q = 4 * hr + 2 * i + (kg & 1);
    __builtin_amdgcn_raw_buffer_store_b128(swap16(o[2 * i], o[2 * i + 1]), rs, ((2 * b + ch) * L_ + 256 * q + 16 * r16 + 8 * (kg >> 1)) * 2, 0, 16);
  }
  WAITVM(0);
  __syncthreads();
  if (tid == 0) __hip_atomic_fetch_add((gu32*)(ws + WS_CNT + (b >> 5) * 128), 1u, __ATOMIC_RELAXED, __HIP_MEMORY_SCOPE_AGENT);
  STAMP(ws, 4);
}

constexpr int G2_A = 0, G2_R = 8192, LDS2 = G2_R + 16 * 4096;
static_assert(C_K % 16 == 0 && C_X + 16384 <= LDS2 && LDS2 <= 81920, "lds");
#define G2D 7

DI void gemm2_role(const char* ws, us* out, int gb, unsigned sb) {
  const int tid = threadIdx.x, lane = tid & 63, hi = lane >> 5, r32 = lane & 31;
  const int wv = __builtin_amdgcn_readfirstlane(tid >> 6);
  const int xcd = gb & 7, slot = gb >> 3;
  const int mt = 4 * xcd + (slot & 3), nt = slot >> 2, m0 = mt * 64, n0 = nt * 64;
  const auto hr = __builtin_amdgcn_make_buffer_rsrc((void*)(ws + WS_H), 0, F_ * L_ * 2, 0x00020000);
  const auto wr = __builtin_amdgcn_make_buffer_rsrc((void*)(ws + WS_W2T), 0, F_ * D_ * 2, 0x00020000);
  int ho = (wv * 28 * 64 + mt * 2) * 1024, wo = (wv * 28 * 16 + nt * 2) * 1024;
  auto ldh = [&](int s, int i) { return __builtin_bit_cast(bf8, __builtin_amdgcn_raw_buffer_load_b128(hr, lane * 16 + i * 1024, ho + s * 65536, 0)); };
  auto ldw = [&](int s, int i) { return __builtin_bit_cast(bf8, __builtin_amdgcn_raw_buffer_load_b128(wr, lane * 16 + i * 1024, wo + s * 16384, 0)); };
  STAMP(ws, 0);
  const float g = ((const float*)(ws + WS_G))[m0 + (wv & 1) * 32 + r32];
  bf8 hf[G2D][2], wf[G2D][2];
#pragma unroll
  for (int s = 0; s < G2D; ++s)
#pragma unroll
    for (int i = 0; i < 2; ++i) {
      __builtin_amdgcn_sched_barrier(0);
      wf[s][i] = ldw(s, i);
      __builtin_amdgcn_sched_barrier(0);
      hf[s][i] = ldh(s, i);
    }
  f16v acc[2][2];
#pragma unroll
  for (int i = 0; i < 2; ++i)
#pragma unroll
    for (int j = 0; j < 2; ++j)
#pragma unroll
      for (int r = 0; r < 16; ++r) acc[i][j][r] = 0.f;
  gu32* cnt = (gu32*)(ws + WS_CNT + nt * 128);
  auto mm = [&](int r, bool more) {
    __builtin_amdgcn_sched_barrier(0);
    acc[0][0] = mfma32(wf[r][0], hf[r][0], acc[0][0]);
    acc[0][1] = mfma32(wf[r][0], hf[r][1], acc[0][1]);
    __builtin_amdgcn_sched_barrier(0);
    if (more) wf[r][0] = ldw(r + G2D, 0);
    acc[1][0] = mfma32(wf[r][1], hf[r][0], acc[1][0]);
    __builtin_amdgcn_sched_barrier(0);
    if (more) hf[r][0] = ldh(r + G2D, 0);
    acc[1][1] = mfma32(wf[r][1], hf[r][1], acc[1][1]);
    __builtin_amdgcn_sched_barrier(0);
    if (more) { wf[r][1] = ldw(r + G2D, 1); __builtin_amdgcn_sched_barrier(0); hf[r][1] = ldh(r + G2D, 1); }
  };
  static_assert(28 % G2D == 0, "ring");
#pragma unroll 1
  for (int it = 0; it < 28 / G2D - 1; ++it) {
#pragma unroll
    for (int r = 0; r < G2D; ++r) mm(r, true);
    wo += G2D * 16384; ho += G2D * 65536;
  }
  unsigned seen = __hip_atomic_load(cnt, __ATOMIC_RELAXED, __HIP_MEMORY_SCOPE_AGENT);
#pragma unroll
  for (int r = 0; r < G2D; ++r) mm(r, false);
#pragma unroll
  for (int i = 0; i < 2; ++i)
#pragma unroll
    for (int j = 0; j < 2; ++j) asm volatile("" : "+v"(acc[i][j]));
  STAMP(ws, 2);
  for (unsigned spin = 0; __builtin_amdgcn_readfirstlane(seen) < 32u && spin < (1u << 24); ++spin) {
    __builtin_amdgcn_s_sleep(8);
    seen = __hip_atomic_load(cnt, __ATOMIC_RELAXED, __HIP_MEMORY_SCOPE_AGENT);
  }
  __builtin_amdgcn_fence(__ATOMIC_ACQUIRE, "wavefront");
  STAMP(ws, 3);
  const auto rs = at_rsrc(ws);
  u4 av[2];
#pragma unroll
  for (int i = 0; i < 2; ++i)
    av[i] = __builtin_amdgcn_raw_buffer_load_b128(rs, ((n0 + i * 32 + (tid >> 3)) * L_ + m0 + (tid & 7) * 8) * 2, 0, 16);
#pragma unroll
  for (int t = 0; t < 4; ++t)
#pragma unroll
    for (int c = 0; c < 4; ++c) {
      const f16v a = acc[t >> 1][t & 1];
      *LDSP(f4, sb + G2_R + (t * 4 + wv) * 4096 + c * 1024 + lane * 16) = (f4){a[4 * c], a[4 * c + 1], a[4 * c + 2], a[4 * c + 3]};
    }
#pragma unroll
  for (int i = 0; i < 2; ++i) *LDSP(u4, sb + G2_A + (i * 32 + (tid >> 3)) * 128 + (tid & 7) * 16) = av[i];
  __syncthreads();
  STAMP(ws, 4);
  const float omg = rbf(1.f - g);
  f4 ps[4][4];
  s4 at[4];
#pragma unroll
  for (int c = 0; c < 4; ++c) {
#pragma unroll
    for (int w = 0; w < 4; ++w) ps[c][w] = *LDSP(f4, sb + G2_R + (wv * 4 + w) * 4096 + c * 1024 + lane * 16);
    at[c] = ldstr(sb + G2_A + ((wv >> 1) * 32 + 8 * c + 4 * hi + ((lane >> 2) & 3)) * 128 + ((wv & 1) * 32 + (lane & 16) + 4 * (lane & 3)) * 2);
  }
  __builtin_amdgcn_sched_barrier(0);
  u2 o[4];
#pragma unroll
  for (int c = 0; c < 4; ++c) {
    f4 s = ((ps[c][0] + ps[c][1]) + (ps[c][2] + ps[c][3])) * omg;
    f4 y;
#pragma unroll
    for (int r = 0; r < 4; ++r) y[r] = fmaf(g, bf2f((us)at[c][r]), s[r]);
    o[c] = (u2){pk2(y[0], y[1]), pk2(y[2], y[3])};
  }
#pragma unroll
  for (int i = 0; i < 2; ++i)
    *(u4*)(out + (size_t)(m0 + (wv & 1) * 32 + r32) * D_ + n0 + (wv >> 1) * 32 + 16 * i + 8 * hi) = swap32(o[2 * i], o[2 * i + 1]);
  STAMP(ws, 5);
}

__global__ void __launch_bounds__(256, 2) k_stage2(char* ws, const us* dsk, us* out) {
  __shared__ __attribute__((aligned(16))) char smem[LDS2];
  const unsigned sb = (unsigned)(uintptr_t)(__attribute__((address_space(3))) char*)smem;
  const unsigned ct = code_touch<48>();
  if (blockIdx.x < 256) conv_role(ws, dsk, blockIdx.x, sb);
  else gemm2_role(ws, out, blockIdx.x - 256, sb);
  asm volatile("" :: "v"(ct));
  STAMPEND(ws)
}

extern "C" void kernel_launch(void* const* d_in, const int* in_sizes, int n_in, void* d_out, int out_size,
                              void* d_ws, size_t ws_size, hipStream_t stream) {
  P1 p;
  p.u = (const us*)d_in[0]; p.are = (const us*)d_in[1]; p.aim = (const us*)d_in[2];
  p.bre = (const us*)d_in[3]; p.bim = (const us*)d_in[4]; p.cre = (const us*)d_in[5]; p.cim = (const us*)d_in[6];
  p.ldt = (const us*)d_in[7];
  p.w1 = (const us*)d_in[10]; p.w3 = (const us*)d_in[11]; p.w2 = (const us*)d_in[12]; p.gw = (const us*)d_in[13];
  p.ws = (char*)d_ws;
  k_stage1<<<480, 256, 0, stream>>>(p);
  k_stage2<<<512, 256, 0, stream>>>((char*)d_ws, (const us*)d_in[8], (us*)d_out);
}
```

```cpp
#include <hip/hip_runtime.h>
#include <hip/hip_bf16.h>
#include <stdint.h>

typedef __bf16 bf8 __attribute__((ext_vector_type(8)));
typedef __bf16 bf2 __attribute__((ext_vector_type(2)));
typedef short s4 __attribute__((ext_vector_type(4)));
typedef short s8 __attribute__((ext_vector_type(8)));
typedef float f2 __attribute__((ext_vector_type(2)));
typedef float f4 __attribute__((ext_vector_type(4)));
typedef float f16v __attribute__((ext_vector_type(16)));
typedef unsigned u2 __attribute__((ext_vector_type(2)));
typedef unsigned u4 __attribute__((ext_vector_type(4)));
typedef unsigned short us;
#define DI __device__ __forceinline__
#define LDSP(T, off) ((__attribute__((address_space(3))) T*)(uintptr_t)(off))

constexpr int L_ = 2048, D_ = 512, F_ = 1792;
constexpr int KVB = 2624;
constexpr size_t WS_H = 4096;
constexpr size_t WS_W2T = WS_H + (size_t)F_ * L_ * 2;
constexpr size_t WS_AT = WS_W2T + (size_t)F_ * D_ * 2;
constexpr size_t WS_UT = WS_AT + (size_t)D_ * L_ * 2;
constexpr size_t WS_G = WS_UT + (size_t)D_ * L_ * 2;
constexpr size_t WS_KV = WS_G + (size_t)L_ * 4;

DI float bf2f(unsigned v) { return __uint_as_float(v << 16); }
DI float bfhi(unsigned v) { return __uint_as_float(v & 0xffff0000u); }
DI unsigned pk2(float a, float b) { f2 v = {a, b}; return __builtin_bit_cast(unsigned, __builtin_convertvector(v, bf2)); }
DI float rbf(float a) { return bf2f(pk2(a, a) & 0xffffu); }
DI f4 mfma16(bf8 a, bf8 b, f4 c) { return __builtin_amdgcn_mfma_f32_16x16x32_bf16(a, b, c, 0, 0, 0); }
DI f16v mfma32(bf8 a, bf8 b, f16v c) { return __builtin_amdgcn_mfma_f32_32x32x16_bf16(a, b, c, 0, 0, 0); }
DI bf8 lds128(unsigned off) { return *LDSP(bf8, off); }
DI bf8 cat8(s4 a, s4 b) { return __builtin_bit_cast(bf8, __builtin_shufflevector(a, b, 0, 1, 2, 3, 4, 5, 6, 7)); }
DI s4 ldstr(unsigned off) { return __builtin_amdgcn_ds_read_tr16_b64_v4i16(LDSP(s4, off)); }
DI void glds16(unsigned voff, const void* sbase, unsigned dst) {
  asm volatile("s_mov_b32 m0, %2\n\ts_nop 0\n\tglobal_load_lds_dwordx4 %0, %1" :: "v"(voff), "s"(sbase), "s"(dst) : "memory");
}
#define WAITVM(n) asm volatile("s_waitcnt vmcnt(" #n ")" ::: "memory")
DI void bar() { asm volatile("s_waitcnt lgkmcnt(0)\n\ts_barrier" ::: "memory"); }

#define STAMP(ws, i) do {} while (0)
#define KID 1
#define SNT 0
#define CPF 1
template <int LINES> DI unsigned code_touch() {
  unsigned long long pc = __builtin_amdgcn_s_getpc();
  unsigned v = *(__attribute__((address_space(1))) unsigned*)((pc & ~127ull) + min((int)threadIdx.x, LINES - 1) * 128ull);
  asm volatile("" ::: "memory");
  return v;
}
struct P1 {
  const us *u, *are, *aim, *bre, *bim, *cre, *cim, *ldt, *w1, *w3, *w2, *gw;
  char* ws;
};
DI u4 swap16(u2 a, u2 b) {
  auto x = __builtin_amdgcn_permlane16_swap(a[0], b[0], false, false);
  auto y = __builtin_amdgcn_permlane16_swap(a[1], b[1], false, false);
  return (u4){x[0], y[0], x[1], y[1]};
}

DI u4 swap32(u2 a, u2 b) {
  auto x = __builtin_amdgcn_permlane32_swap(a[0], b[0], false, false);
  auto y = __builtin_amdgcn_permlane32_swap(a[1], b[1], false, false);
  return (u4){x[0], y[0], x[1], y[1]};
}
template <int V> struct IC { static constexpr int v = V; };

constexpr int XS = 16384, WSZ = 16384;
constexpr int G1_X = 0, G1_W = 2 * XS;
constexpr int LDS1 = 81920;
#define EPO 1
#define G1MAP 1
#define ROT 3

DI void gemm1_role(const P1& p, int gb, unsigned sb) {
  const int tid = threadIdx.x, lane = tid & 63;
  const int wv = __builtin_amdgcn_readfirstlane(tid >> 6);
  const int xcd = gb & 7, slot = gb >> 3;
  const int mt = 2 * xcd + (slot & 1), nt = slot >> 1;
  const int m0 = mt * 128, n0 = nt * 128;
  const int rot = ROT == 1 ? (slot & 7) : (ROT == 2 ? ((slot >> 1) & 7) : (ROT == 3 ? xcd : 0));
  STAMP(p.ws, 0);
  unsigned xoff[2];
#pragma unroll
  for (int i = 0; i < 2; ++i) xoff[i] = (lane >> 3) * (D_ * 2) + (((lane & 7) ^ (4 * i + (lane >> 4))) << 4);
  const us* xs = p.u + (size_t)(m0 + wv * 32) * D_;
  auto issueX1 = [&](int t, int i) { glds16(xoff[i & 1], xs + ((t + rot) & 7) * 64 + i * 8 * D_, sb + G1_X + (t & 1) * XS + wv * 4096 + i * 1024); };
#pragma unroll
  for (int i = 0; i < 4; ++i) issueX1(0, i);
  const unsigned woff = (lane >> 4) * (F_ * 2) + (((lane & 15) ^ ((lane >> 4) << 2)) << 4);
  const us* ws1 = p.w1 + wv * 8 * F_ + n0;
  const us* ws3 = p.w3 + wv * 8 * F_ + n0;
  auto issueW1 = [&](int s, int slot3, int i) {
    const int ko = (((s + 2 * rot) & 15) * 32 + (i & 1) * 4) * F_;
    glds16(woff, ((i >> 1) ? ws3 : ws1) + ko, sb + G1_W + slot3 + (i >> 1) * 8192 + wv * 2048 + (i & 1) * 1024);
  };
#pragma unroll
  for (int i = 0; i < 4; ++i) issueW1(0, 0, i);
#pragma unroll
  for (int i = 0; i < 4; ++i) issueW1(1, WSZ, i);
#pragma unroll
  for (int i = 0; i < 4; ++i) issueX1(1, i);
#pragma unroll
  for (int i = 0; i < 4; ++i) issueW1(2, 2 * WSZ, i);

  const int wm = wv & 1, wn = wv >> 1, hi = lane >> 5, r32 = lane & 31, q = (lane >> 2) & 3;
  unsigned xb[4], wb[2];
#pragma unroll
  for (int c = 0; c < 4; ++c) xb[c] = sb + G1_X + (wm * 64 + r32) * 128 + (((c * 2 + hi) ^ ((r32 >> 1) & 7)) << 4);
#pragma unroll
  for (int j = 0; j < 2; ++j) wb[j] = sb + G1_W + (8 * hi + q) * 256 + (((wn * 2 + j) ^ q) << 6) + (lane & 16) * 2 + (lane & 3) * 8;
  auto rdW = [&](int so, int f) {
    unsigned a = wb[(f >> 1) & 1] + so + (f & 1) * 8192 + (f >> 2) * 4096;
    return cat8(ldstr(a), ldstr(a + 1024));
  };
  f16v acc[4][2];
#pragma unroll
  for (int j = 0; j < 4; ++j)
#pragma unroll
    for (int i = 0; i < 2; ++i)
#pragma unroll
      for (int r = 0; r < 16; ++r) acc[j][i][r] = 0.f;
  bf8 xf[2][4], wf[2][8];
  WAITVM(12);
  bar();
  STAMP(p.ws, 1);
#pragma unroll
  for (int i = 0; i < 4; ++i) xf[0][i] = lds128(xb[i >> 1] + (i & 1) * 4096);
#pragma unroll
  for (int f = 0; f < 8; ++f) wf[0][f] = rdW(0, f);
  int cur = 0;
  auto body = [&](int it, auto NG) {
#pragma unroll
    for (int h = 0; h < 2; ++h) {
      const int s = 2 * it + h;
      const bool go = h < NG.v, last = NG.v == 0 && h;
      if (!last) {
        if (h) WAITVM(4);
        else if (NG.v) WAITVM(8);
        else WAITVM(0);
        bar();
      }
      const int nxt = cur == 2 * WSZ ? 0 : cur + WSZ;
      if (h) { xb[0] ^= XS; xb[1] ^= XS; }
      __builtin_amdgcn_sched_barrier(0);
#pragma unroll
      for (int f = 0; f < (last && EPO ? 0 : 8); ++f) {
        acc[f & 3][0] = mfma32(wf[h][f], xf[h][(f >> 2) * 2], acc[f & 3][0]);
        __builtin_amdgcn_sched_barrier(0);
        if (!last) {
          if (f < 4) xf[h ^ 1][f] = lds128(xb[(h ^ 1) * 2 + (f >> 1)] + (f & 1) * 4096);
          wf[h ^ 1][f] = rdW(nxt, f);
        }
        __builtin_amdgcn_sched_barrier(0);
        acc[f & 3][1] = mfma32(wf[h][f], xf[h][(f >> 2) * 2 + 1], acc[f & 3][1]);
        __builtin_amdgcn_sched_barrier(0);
        if (go) {
          if (h) { if (f < 4) issueW1(s + 3, cur, f); else issueX1(it + 2, f - 4); }
          else if (!(f & 1)) issueW1(s + 3, cur, f >> 1);
        }
        __builtin_amdgcn_sched_barrier(0);
      }
      if (!h) { xb[2] ^= XS; xb[3] ^= XS; }
      cur = nxt;
    }
  };
#pragma unroll 1
  for (int it = 0; it < 6; ++it) body(it, IC<2>());
  body(6, IC<1>());
  body(7, IC<0>());
  STAMP(p.ws, 7);
  auto lm = [&](int n) {
    const int j = n >> 3, i = (n >> 2) & 1, kk = (n >> 1) & 1, a = 2 * j + (n & 1);
    acc[a][i] = mfma32(wf[1][kk * 4 + a], xf[1][kk * 2 + i], acc[a][i]);
  };
  if (EPO) {
#pragma unroll
    for (int n = 0; n < 4; ++n) lm(n);
  }
  char* hp = p.ws + WS_H + ((nt * 8 + wn * 4) * 64 + mt * 4 + wm * 2) * 1024 + lane * 16;
#pragma unroll
  for (int j = 0; j < 2; ++j)
#pragma unroll
    for (int i = 0; i < 2; ++i)
#pragma unroll
      for (int c = 0; c < 2; ++c) {
        u2 o[2];
#pragma unroll
        for (int r = 0; r < 4; ++r) {
          if (EPO && j * 16 + i * 8 + c * 4 + r < 12) {
            __builtin_amdgcn_sched_barrier(0);
            lm(4 + i * 8 + c * 4 + r);
            __builtin_amdgcn_sched_barrier(0);
          }
          f2 g = {acc[2 * j][i][8 * c + 2 * r], acc[2 * j][i][8 * c + 2 * r + 1]};
          f2 up = {acc[2 * j + 1][i][8 * c + 2 * r], acc[2 * j + 1][i][8 * c + 2 * r + 1]};
          f2 e = g * -1.4426950408889634f;
          e = (f2){__builtin_amdgcn_exp2f(e.x), __builtin_amdgcn_exp2f(e.y)} + 1.f;
          e = (g * (f2){__builtin_amdgcn_rcpf(e.x), __builtin_amdgcn_rcpf(e.y)}) * up;
          o[r >> 1][r & 1] = pk2(e.x, e.y);
        }
        *(u4*)(hp + i * 1024 + (2 * j + c) * 65536) = swap32(o[0], o[1]);
      }
  STAMP(p.ws, 8);
}

constexpr int TROW = 544, TCH = 53 * TROW;
constexpr int S_X = 0, XROW = 144;
constexpr int S_KV = 64 * XROW;
constexpr int S_T = S_KV + KVB;
static_assert(S_T % 16 == 0 && S_T + TCH + 64 * TROW <= LDS1, "lds");

DI f2 cmul(f2 a, f2 b) { return (f2){fmaf(a.x, b.x, -a.y * b.y), fmaf(a.x, b.y, a.y * b.x)}; }
DI f2 cexpl(float ar, float ai, float l) {
  float e = __expf(ar * l);
  float x = ai * l;
  float r = x * 0.15915494309189535f;
  float err = fmaf(x, 0.15915494309189535f, -r) + x * 6.4206383e-9f;
  float f = __builtin_amdgcn_fractf(r) + err;
  return (f2){e * __builtin_amdgcn_cosf(f), e * __builtin_amdgcn_sinf(f)};
}

DI float dpp_sum(float s) {
#define DPPADD(ctl, rm) s += __uint_as_float(__builtin_amdgcn_update_dpp(0u, __float_as_uint(s), ctl, rm, 0xf, false))
  DPPADD(0x111, 0xf); DPPADD(0x112, 0xf); DPPADD(0x114, 0xf); DPPADD(0x118, 0xf);
  DPPADD(0x142, 0xa); DPPADD(0x143, 0xc);
  return s;
}

DI void side_role(const P1& p, int b, unsigned sb) {
  const int tid = threadIdx.x, lane = tid & 63;
  const int wv = __builtin_amdgcn_readfirstlane(tid >> 6);
  const int ch = wv >> 1, n = tid & 127, kg = lane >> 4, r16 = lane & 15;
  STAMP(p.ws, 0);
#define SPRIO 3
  __builtin_amdgcn_s_setprio(SPRIO);
  const int pi = b * 256 + tid;
  const float Are = bf2f(p.are[pi]), Aim = bf2f(p.aim[pi]), Bre = bf2f(p.bre[pi]), Bim = bf2f(p.bim[pi]);
  const float Cre = bf2f(p.cre[pi]), Cim = bf2f(p.cim[pi]);
  const float ldt = bf2f(p.ldt[2 * b + ch]);
  const int l0 = (b >> 3) * 64, c0 = (b & 7) * 64;
  u4 ut[2], gx[2], gwv;
#pragma unroll
  for (int i = 0; i < 2; ++i) ut[i] = *(const u4*)(p.u + (size_t)(l0 + i * 32 + (tid >> 3)) * D_ + c0 + (tid & 7) * 8);
  gwv = *(const u4*)(p.gw + lane * 8);
#pragma unroll
  for (int i = 0; i < 2; ++i) gx[i] = *(const u4*)(p.u + (size_t)(b * 8 + wv * 2 + i) * D_ + lane * 8);
  unsigned w2v[8];
  const int bw = min(b, 223);
#pragma unroll
  for (int i = 0; i < 8; ++i) w2v[i] = *(const unsigned*)(p.w2 + (size_t)(bw * 8 + i) * D_ + tid * 2);
  if (tid < 4) *LDSP(unsigned, sb + S_KV + tid * 4) = 0u;
#pragma unroll
  for (int i = 0; i < 2; ++i)
    __builtin_amdgcn_raw_buffer_store_b128((u4){~0u, ~0u, ~0u, ~0u}, __builtin_amdgcn_make_buffer_rsrc(p.ws + WS_AT, 0, D_ * L_ * 2, 0x00020000),
                                           b * 8192 + i * 4096 + tid * 16, 0, SNT);
  {
    const float dt = rbf(__expf(ldt));
    const float ar = dt * Are, ai = dt * Aim;
    const f2 W = cmul((f2){Cre, Cim}, (f2){dt * Bre, dt * Bim});
    const f2 e1 = cexpl(ar, ai, 1.f), e8 = cexpl(ar, ai, 8.f), e128 = cexpl(ar, ai, 128.f), e256 = cexpl(ar, ai, 256.f);
    const unsigned tb = sb + S_T + ch * TCH + n * 4;
    f2 lo = {1.f, 0.f};
#pragma unroll 2
    for (int i = 0; i < 16; ++i) {
      f2 hi = cmul(lo, e128);
      *LDSP(unsigned, tb + i * TROW) = pk2(lo.x, lo.y);
      *LDSP(unsigned, tb + (16 + i) * TROW) = pk2(hi.x, hi.y);
      lo = cmul(lo, e8);
    }
    f2 a[8];
    a[0] = W;
#pragma unroll
    for (int i = 1; i < 8; ++i) a[i] = cmul(a[i - 1], e1);
#define PUTA(row, v) *LDSP(unsigned, tb + (32 + (row)) * TROW) = pk2((v).x, -(v).y)
#pragma unroll
    for (int i = 0; i < 8; ++i) PUTA(i, a[i]);
#pragma unroll
    for (int i = 0; i < 4; ++i) { a[2 * i] = cmul(a[2 * i], e256); PUTA(8 + i, a[2 * i]); }
    a[0] = cmul(a[0], e256); a[4] = cmul(a[4], e256); PUTA(12, a[0]); PUTA(13, a[4]);
    a[0] = cmul(a[0], e256); a[4] = cmul(a[4], e256); PUTA(14, a[0]); PUTA(15, a[4]);
#pragma unroll
    for (int i = 0; i < 5; ++i) { a[0] = cmul(a[0], e256); PUTA(16 + i, a[0]); }
  }
  STAMP(p.ws, 1);
#pragma unroll
  for (int i = 0; i < 2; ++i) *LDSP(u4, sb + S_X + (i * 32 + (tid >> 3)) * XROW + (tid & 7) * 16) = ut[i];
  __syncthreads();
  STAMP(p.ws, 2);
  {
    f4 k0 = {0.f, 0.f, 0.f, 0.f}, k1 = k0;
    const unsigned tb = sb + S_T + ch * TCH + r16 * TROW + kg * 16;
    const unsigned lb = tb + (wv & 1) * 16 * TROW;
#pragma unroll
    for (int ks = 0; ks < 8; ++ks) {
      bf8 bl = lds128(lb + ks * 64);
      k0 = mfma16(lds128(tb + 32 * TROW + ks * 64), bl, k0);
      k1 = mfma16(lds128(tb + 48 * TROW + ks * 64), bl, k1);
    }
    const int c = (wv & 1) * 16 + r16;
    const unsigned kv = sb + S_KV + 16 + ch * 2;
    int i0 = kg < 2 ? 8 * c + 4 * kg : (kg == 2 ? 256 + 4 * c : 384 + 2 * c);
    int i1 = kg < 3 ? i0 + 2 : i0 + 64;
    unsigned pa = pk2(k0[0], k0[1]), pb = pk2(k0[2], k0[3]);
    *LDSP(us, kv + i0 * 4) = (us)pa; *LDSP(us, kv + i0 * 4 + 4) = (us)(pa >> 16);
    *LDSP(us, kv + i1 * 4) = (us)pb; *LDSP(us, kv + i1 * 4 + 4) = (us)(pb >> 16);
    pa = pk2(k1[0], k1[1]); pb = pk2(k1[2], k1[3]);
    int i2 = kg == 0 ? 512 + c : (kg == 1 && c == 0 ? 640 : 641), st = kg == 0 ? 128 : 0;
    *LDSP(us, kv + i2 * 4) = (us)pa;
    i2 = kg == 0 ? i2 : 641;
    *LDSP(us, kv + i2 * 4 + st) = (us)(pa >> 16); *LDSP(us, kv + i2 * 4 + 2 * st) = (us)pb; *LDSP(us, kv + i2 * 4 + 3 * st) = (us)(pb >> 16);
  }
  STAMP(p.ws, 3);
#pragma unroll
  for (int i = 0; i < 2; ++i) {
    unsigned a = sb + S_X + (i * 32 + kg * 8 + (r16 >> 2)) * XROW + (wv * 16 + (lane & 3) * 4) * 2;
    *(u4*)(p.ws + WS_UT + ((c0 + wv * 16 + r16) * L_ + l0 + i * 32 + kg * 8) * 2) = __builtin_bit_cast(u4, cat8(ldstr(a), ldstr(a + 4 * XROW)));
  }
#pragma unroll
  for (int i = 0; i < 2; ++i) {
    float s = 0.f;
#pragma unroll
    for (int e = 0; e < 4; ++e) {
      unsigned pr = pk2(bf2f(gx[i][e] & 0xffffu) * bf2f(gwv[e] & 0xffffu), bfhi(gx[i][e]) * bfhi(gwv[e]));
      s += bf2f(pr & 0xffffu) + bfhi(pr);
    }
    s = rbf(dpp_sum(s));
    if (lane == 63) ((float*)(p.ws + WS_G))[b * 8 + wv * 2 + i] = rbf(__builtin_amdgcn_rcpf(1.f + __expf(-s)));
  }
  if (b < 224) {
    u4 lo, hi;
#pragma unroll
    for (int e = 0; e < 4; ++e) {
      lo[e] = __builtin_amdgcn_perm(w2v[2 * e + 1], w2v[2 * e], 0x05040100);
      hi[e] = __builtin_amdgcn_perm(w2v[2 * e + 1], w2v[2 * e], 0x07060302);
    }
    const int o = ((((b >> 1) * 16 + (tid >> 4)) * 2 + (b & 1)) * 32 + (tid & 15) * 2) * 16;
    *(u4*)(p.ws + WS_W2T + o) = lo;
    *(u4*)(p.ws + WS_W2T + o + 16) = hi;
  }
  __syncthreads();
  if (tid < KVB / 16) *(u4*)(p.ws + WS_KV + b * KVB + tid * 16) = *LDSP(u4, sb + S_KV + tid * 16);
  STAMP(p.ws, 4);
}

#define STAMPEND(ws)
__global__ void __launch_bounds__(256, 2) k_stage1(P1 p) {
  __shared__ __attribute__((aligned(16))) char smem[LDS1];
  const unsigned sb = (unsigned)(uintptr_t)(__attribute__((address_space(3))) char*)smem;
  const unsigned ct = code_touch<134>();
  if (blockIdx.x < 224) gemm1_role(p, blockIdx.x, sb);
  else side_role(p, blockIdx.x - 224, sb);
  asm volatile("" :: "v"(ct));
  STAMPEND(p.ws)
}

#undef KID
#define KID 2
constexpr int C_U = 0, UPH = 1184, UCH = 4 * UPH;
constexpr int C_KV = 2 * UCH, C_K = C_KV + KVB;
constexpr int KCP = 4128, KCH = 4 * KCP, C_X = C_K + 2 * KCH + 1024;
DI __amdgpu_buffer_rsrc_t at_rsrc(const char* ws) { return __builtin_amdgcn_make_buffer_rsrc((void*)(ws + WS_AT), 0, D_ * L_ * 2, 0x00020000); }

DI void conv_role(char* ws, const us* dskp, int b, unsigned sb) {
  const int tid = threadIdx.x, lane = tid & 63;
  const int wv = __builtin_amdgcn_readfirstlane(tid >> 6);
  const int ch = wv >> 1, kg = lane >> 4, r16 = lane & 15;
  STAMP(ws, 0);
#define CPRIO 0
  __builtin_amdgcn_s_setprio(CPRIO);
  u4 kvv = *(const u4*)(ws + WS_KV + (size_t)b * KVB + min(tid, KVB / 16 - 1) * 16);
  u4 uv[2];
#pragma unroll
  for (int c = 0; c < 2; ++c) uv[c] = *(const u4*)(ws + WS_UT + (size_t)(2 * b + c) * L_ * 2 + tid * 16);
  const float dsk = bf2f(dskp[2 * b + ch]);
  unsigned kidx[11];
#pragma unroll
  for (int e = 0; e < 11; ++e) {
    int j = 8 * tid - 3 + e;
    int ix = (int)(pk2((float)j, 0.f) & 0xffffu) - 0x4280;
    kidx[e] = sb + C_KV + 16 + (j < 128 ? j : ix) * 4;
  }
  *LDSP(unsigned, sb + C_U + (tid >> 5) * UPH + (tid & 31) * 4) = 0u;
  if (tid < 64) *LDSP(unsigned, sb + C_K + (tid >> 3) * KCP + (tid & 7) * 4) = 0u;
  *LDSP(u4, sb + C_KV + min(tid, KVB / 16 - 1) * 16) = kvv;
  __syncthreads();
  STAMP(ws, 1);
  {
    unsigned v[11];
#pragma unroll
    for (int e = 0; e < 11; ++e) v[e] = *LDSP(unsigned, kidx[e]);
#pragma unroll
    for (int c = 0; c < 2; ++c)
#pragma unroll
      for (int r = 0; r < 4; ++r)
        *LDSP(unsigned, sb + C_U + c * UCH + r * UPH + 128 + tid * 4) =
            __builtin_amdgcn_perm(uv[c][2 + (r >> 1)], uv[c][r >> 1], (r & 1) ? 0x07060302 : 0x05040100);
#pragma unroll
    for (int c = 0; c < 2; ++c) {
      unsigned pr[10];
#pragma unroll
      for (int i = 0; i < 10; ++i) pr[i] = __builtin_amdgcn_perm(v[i + 1], v[i], c ? 0x07060302 : 0x05040100);
      unsigned a = sb + C_K + c * KCH + 32 + tid * 16;
#pragma unroll
      for (int r = 0; r < 4; ++r) *LDSP(u4, a + r * KCP) = (u4){pr[3 - r], pr[5 - r], pr[7 - r], pr[9 - r]};
    }
  }
  __syncthreads();
  STAMP(ws, 2);
  f4 acc[8];
#pragma unroll
  for (int i = 0; i < 8; ++i) acc[i] = (f4){0.f, 0.f, 0.f, 0.f};
  const int hr = wv & 1;
  unsigned kb = sb + C_K + ch * KCH + 2 * hr * KCP + (16 + 112 - 4 * (8 * kg + 4 + (r16 >> 2)) + 4 * (lane & 3)) * 2;
  unsigned ub = sb + C_U + ch * UCH + 2 * hr * UPH + (64 - 32 - 28 + 4 * r16 + 8 * kg) * 2;
  auto rdU = [&](int o) { return cat8(*LDSP(volatile s4, ub + o), *LDSP(volatile s4, ub + o + 8)); };
  auto rdK = [&](int o) { return cat8(ldstr(kb + o + 32), ldstr(kb + o)); };
  bf8 uf[16], kf[4];
#pragma unroll
  for (int x = 0; x < 3; ++x) kf[x] = rdK(x * 256);
#pragma unroll
  for (int s = 15; s >= 0; s -= 2) uf[s] = rdU(s * 64);
#pragma unroll
  for (int s = 14; s >= 0; s -= 2) uf[s] = rdU(s * 64);
  __builtin_amdgcn_sched_barrier(0);
#pragma unroll 1
  for (int rho = 0; rho < 2; ++rho) {
#pragma unroll
    for (int t = 0; t < 16; ++t) {
      kf[(t + 3) & 3] = rdK(t < 13 ? (t + 3) * 256 : KCP + (t - 13) * 256);
#pragma unroll
      for (int q = 7; q >= 0; --q)
        if (t <= 2 * q + 1) acc[q] = mfma16(kf[t & 3], uf[2 * q - t + 1], acc[q]);
      uf[15 - t] = rdU(UPH + (15 - t) * 64);
      __builtin_amdgcn_sched_barrier(0);
    }
    kb += KCP; ub += UPH;
  }
  STAMP(ws, 3);
  const unsigned xb = sb + C_X + ch * 8192 + lane * 16;
#pragma unroll
  for (int i = 0; i < 4; ++i) {
    f4 lo = acc[i], hi = acc[4 + i];
    asm volatile("" : "+v"(lo), "+v"(hi));
    *LDSP(f4, xb + (hr * 4 + i) * 1024) = hr ? lo : hi;
    acc[i] = hr ? hi : lo;
  }
  float uu[4][4];
#pragma unroll
  for (int i = 0; i < 4; ++i)
#pragma unroll
    for (int r = 0; r < 4; ++r) uu[i][r] = dsk * bf2f(*LDSP(us, sb + C_U + ch * UCH + r * UPH + (64 + 64 * (4 * hr + i) + 4 * r16 + kg) * 2));
  __syncthreads();
  u2 o[4];
#pragma unroll
  for (int i = 0; i < 4; ++i) {
    f4 y = *LDSP(f4, xb + ((hr ^ 1) * 4 + i) * 1024) + acc[i];
    o[i] = (u2){pk2(y[0] + uu[i][0], y[1] + uu[i][1]), pk2(y[2] + uu[i][2], y[3] + uu[i][3])};
  }
  const auto rs = at_rsrc(ws);
#pragma unroll
  for (int i = 0; i < 2; ++i) {
    int q = 4 * hr + 2 * i + (kg & 1);
    u4 v = swap16(o[2 * i], o[2 * i + 1]);
#pragma unroll
    for (int e = 0; e < 4; ++e) v[e] = min(v[e], 0xfffffffeu);
    __builtin_amdgcn_raw_buffer_store_b128(v, rs, ((2 * b + ch) * L_ + 256 * q + 16 * r16 + 8 * (kg >> 1)) * 2, 0, 16);
  }
  STAMP(ws, 4);
}

constexpr int G2_A = 0, G2_R = 8192, LDS2 = G2_R + 16 * 4096;
static_assert(C_K % 16 == 0 && C_X + 16384 <= LDS2 && LDS2 <= 81920, "lds");
#define GPRIO 0
#define PSL 2
#define G2D 4

DI void gemm2_role(const char* ws, us* out, int gb, unsigned sb) {
  const int tid = threadIdx.x, lane = tid & 63, hi = lane >> 5, r32 = lane & 31;
  const int wv = __builtin_amdgcn_readfirstlane(tid >> 6);
  const int xcd = gb & 7, slot = gb >> 3;
  const int mt = 4 * xcd + (slot & 3), nt = slot >> 2, m0 = mt * 64, n0 = nt * 64;
  const auto hr = __builtin_amdgcn_make_buffer_rsrc((void*)(ws + WS_H), 0, F_ * L_ * 2, 0x00020000);
  const auto wr = __builtin_amdgcn_make_buffer_rsrc((void*)(ws + WS_W2T), 0, F_ * D_ * 2, 0x00020000);
  int ho = (wv * 28 * 64 + mt * 2) * 1024, wo = (wv * 28 * 16 + nt * 2) * 1024;
  auto ldh = [&](int s, int i) { return __builtin_bit_cast(bf8, __builtin_amdgcn_raw_buffer_load_b128(hr, lane * 16 + i * 1024, ho + s * 65536, 0)); };
  auto ldw = [&](int s, int i) { return __builtin_bit_cast(bf8, __builtin_amdgcn_raw_buffer_load_b128(wr, lane * 16 + i * 1024, wo + s * 16384, 0)); };
  STAMP(ws, 0);
  __builtin_amdgcn_s_setprio(GPRIO);
  const float g = ((const float*)(ws + WS_G))[m0 + (wv & 1) * 32 + r32];
  bf8 hf[G2D][2], wf[G2D][2];
#pragma unroll
  for (int s = 0; s < G2D; ++s)
#pragma unroll
    for (int i = 0; i < 2; ++i) {
      __builtin_amdgcn_sched_barrier(0);
      wf[s][i] = ldw(s, i);
      __builtin_amdgcn_sched_barrier(0);
      hf[s][i] = ldh(s, i);
    }
  f16v acc[2][2];
#pragma unroll
  for (int i = 0; i < 2; ++i)
#pragma unroll
    for (int j = 0; j < 2; ++j)
#pragma unroll
      for (int r = 0; r < 16; ++r) acc[i][j][r] = 0.f;
  auto mm = [&](int r, bool more) {
    __builtin_amdgcn_sched_barrier(0);
    acc[0][0] = mfma32(wf[r][0], hf[r][0], acc[0][0]);
    acc[0][1] = mfma32(wf[r][0], hf[r][1], acc[0][1]);
    __builtin_amdgcn_sched_barrier(0);
    if (more) wf[r][0] = ldw(r + G2D, 0);
    acc[1][0] = mfma32(wf[r][1], hf[r][0], acc[1][0]);
    __builtin_amdgcn_sched_barrier(0);
    if (more) hf[r][0] = ldh(r + G2D, 0);
    acc[1][1] = mfma32(wf[r][1], hf[r][1], acc[1][1]);
    __builtin_amdgcn_sched_barrier(0);
    if (more) { wf[r][1] = ldw(r + G2D, 1); __builtin_amdgcn_sched_barrier(0); hf[r][1] = ldh(r + G2D, 1); }
  };
  static_assert(28 % G2D == 0, "ring");
#pragma unroll 1
  for (int it = 0; it < 28 / G2D - 1; ++it) {
#pragma unroll
    for (int r = 0; r < G2D; ++r) mm(r, true);
    wo += G2D * 16384; ho += G2D * 65536;
  }
  const auto rs = at_rsrc(ws);
  u4 av[2];
  auto lda = [&]() {
    asm volatile("" ::: "memory");
#pragma unroll
    for (int i = 0; i < 2; ++i)
      av[i] = __builtin_amdgcn_raw_buffer_load_b128(rs, ((i * 16 + (lane >> 2)) * L_ + (lane & 3) * 8) * 2, ((n0 + (wv >> 1) * 32) * L_ + m0 + (wv & 1) * 32) * 2, 16);
    asm volatile("" ::: "memory");
  };
  auto missing = [&]() {
    unsigned m = max(max(max(av[0][0], av[0][1]), max(av[0][2], av[0][3])), max(max(av[1][0], av[1][1]), max(av[1][2], av[1][3])));
    return __builtin_amdgcn_ballot_w64(m == ~0u) != 0;
  };
  __builtin_amdgcn_sched_barrier(0);
  lda();
#pragma unroll
  for (int r = 0; r < G2D; ++r) mm(r, false);
  STAMP(ws, 2);
#pragma unroll
  for (int t = 0; t < 4; ++t)
#pragma unroll
    for (int c = 0; c < 4; ++c) {
      const f16v a = acc[t >> 1][t & 1];
      *LDSP(f4, sb + G2_R + (t * 4 + wv) * 4096 + c * 1024 + lane * 16) = (f4){a[4 * c], a[4 * c + 1], a[4 * c + 2], a[4 * c + 3]};
    }
  if (missing()) lda();
  STAMP(ws, 3);
  bar();
  const float omg = rbf(1.f - g);
  f4 ps[4][4];
  s4 at[4];
#pragma unroll
  for (int c = 0; c < 4; ++c) {
#pragma unroll
    for (int w = 0; w < 4; ++w) ps[c][w] = *LDSP(f4, sb + G2_R + (wv * 4 + w) * 4096 + c * 1024 + lane * 16);
    ps[c][0] = ((ps[c][0] + ps[c][1]) + (ps[c][2] + ps[c][3])) * omg;
  }
  for (unsigned spin = 0; missing() && spin < (1u << 22); ++spin) {
    __builtin_amdgcn_s_sleep(PSL);
    lda();
  }
  STAMP(ws, 4);
#pragma unroll
  for (int i = 0; i < 2; ++i) *LDSP(u4, sb + G2_A + wv * 2048 + i * 1024 + lane * 16) = av[i];
  asm volatile("" ::: "memory");
#pragma unroll
  for (int c = 0; c < 4; ++c) at[c] = ldstr(sb + G2_A + wv * 2048 + (8 * c + 4 * hi + ((lane >> 2) & 3)) * 64 + ((lane & 16) + 4 * (lane & 3)) * 2);
  u2 o[4];
#pragma unroll
  for (int c = 0; c < 4; ++c) {
    f4 s = ps[c][0];
    f4 y;
#pragma unroll
    for (int r = 0; r < 4; ++r) y[r] = fmaf(g, bf2f((us)at[c][r]), s[r]);
    o[c] = (u2){pk2(y[0], y[1]), pk2(y[2], y[3])};
  }
#pragma unroll
  for (int i = 0; i < 2; ++i)
    *(u4*)(out + (size_t)(m0 + (wv & 1) * 32 + r32) * D_ + n0 + (wv >> 1) * 32 + 16 * i + 8 * hi) = swap32(o[2 * i], o[2 * i + 1]);
  STAMP(ws, 5);
}

__global__ void __launch_bounds__(256, 2) k_stage2(char* ws, const us* dsk, us* out) {
  __shared__ __attribute__((aligned(16))) char smem[LDS2];
  const unsigned sb = (unsigned)(uintptr_t)(__attribute__((address_space(3))) char*)smem;
  const unsigned ct = code_touch<48>();
  if (blockIdx.x < 256) conv_role(ws, dsk, blockIdx.x, sb);
  else gemm2_role(ws, out, blockIdx.x - 256, sb);
  asm volatile("" :: "v"(ct));
  STAMPEND(ws)
}

extern "C" void kernel_launch(void* const* d_in, const int* in_sizes, int n_in, void* d_out, int out_size,
                              void* d_ws, size_t ws_size, hipStream_t stream) {
  P1 p;
  p.u = (const us*)d_in[0]; p.are = (const us*)d_in[1]; p.aim = (const us*)d_in[2];
  p.bre = (const us*)d_in[3]; p.bim = (const us*)d_in[4]; p.cre = (const us*)d_in[5]; p.cim = (const us*)d_in[6];
  p.ldt = (const us*)d_in[7];
  p.w1 = (const us*)d_in[10]; p.w3 = (const us*)d_in[11]; p.w2 = (const us*)d_in[12]; p.gw = (const us*)d_in[13];
  p.ws = (char*)d_ws;
  k_stage1<<<480, 256, 0, stream>>>(p);
  k_stage2<<<512, 256, 0, stream>>>((char*)d_ws, (const us*)d_in[8], (us*)d_out);
}
```
